```python
import torch

def _softmax(x, axis=-1):
    return torch.softmax(x, dim=axis)
def _gelu(x):
    return torch.nn.functional.gelu(x, approximate="tanh")
def _silu(x):
    return torch.nn.functional.silu(x)
def _sigmoid(x):
    return torch.sigmoid(x)
def _layernorm(x, axis=-1, eps=1e-5):
    m = x.mean(dim=axis, keepdim=True)
    v = x.var(dim=axis, keepdim=True, unbiased=False)
    return (x - m) / torch.sqrt(v + eps)
def _scatter_add(tgt, idx, src, axis=0):
    return tgt.clone().index_add_(axis, idx, src)
def _segment_sum(src, seg, n):
    out = torch.zeros((n,) + src.shape[1:], dtype=src.dtype)
    return out.index_add_(0, seg, src)
def _pad(x, pw):
    flat = tuple(v for pair in reversed(pw) for v in pair)
    return torch.nn.functional.pad(x, flat)

kernel_name = 'sequential_preLN__grouped_kv_mqa__mixer_s_mlp__prompt'

def setup_inputs():

    _w = (lambda a: (lambda t: t.to(torch.bfloat16) if t.is_floating_point() else t)(torch.as_tensor(a)))
    def _A_setup():
        B, S, D, H = 1, 2048, 640, 32
        Dh = D // H
        return {
            "x": _w((lambda *s: torch.randn(*s, dtype=torch.bfloat16) * 0.05)(B, S, D)),
            "wq": _w((lambda *s: torch.randn(*s, dtype=torch.bfloat16) * 0.05)(D, H * Dh)),
            "wk": _w((lambda *s: torch.randn(*s, dtype=torch.bfloat16) * 0.05)(D, Dh)),
            "wv": _w((lambda *s: torch.randn(*s, dtype=torch.bfloat16) * 0.05)(D, Dh)),
            "wo": _w((lambda *s: torch.randn(*s, dtype=torch.bfloat16) * 0.05)(H * Dh, D)),
        }
    _A_in = _A_setup()

    def _B_setup():
        B, S, D, F = 1, 2048, 640, 1728
        return {
            "x":  _w((lambda *s: torch.randn(*s, dtype=torch.bfloat16) * 0.05)(B, S, D)),
            "w1": _w((lambda *s: torch.randn(*s, dtype=torch.bfloat16) * 3.2)(D, D) * (D ** -0.5)),
            "w2": _w((lambda *s: torch.randn(*s, dtype=torch.bfloat16) * 3.2)(D, D) * (D ** -0.5)),
            "w3": _w((lambda *s: torch.randn(*s, dtype=torch.bfloat16) * 3.2)(D, F) * (D ** -0.5)),
            "w4": _w((lambda *s: torch.randn(*s, dtype=torch.bfloat16) * 3.2)(F, D) * (F ** -0.5)),
        }
    _B_in = _B_setup()

    return {
        "A_x": _A_in["x"],
        "A_wq": _A_in["wq"],
        "A_wk": _A_in["wk"],
        "A_wv": _A_in["wv"],
        "A_wo": _A_in["wo"],
        "B_x": _B_in["x"],
        "B_w1": _B_in["w1"],
        "B_w2": _B_in["w2"],
        "B_w3": _B_in["w3"],
        "B_w4": _B_in["w4"],
    }

def reference(A_x, A_wq, A_wk, A_wv, A_wo, B_x, B_w1, B_w2, B_w3, B_w4):
    def _A_ref(x, wq, wk, wv, wo):
        B, S, D = x.shape
        H = 32
        Dh = D // H
        q = (lambda x, *d: x.permute(*d))((x @ wq).reshape(B, S, H, Dh), 0, 2, 1, 3)
        k = x @ wk
        v = x @ wv
        scores = torch.einsum("bhqd,bkd->bhqk", q, k) * (Dh ** -0.5)
        p = _softmax(scores, axis=-1)
        o = torch.einsum("bhqk,bkd->bhqd", p, v)
        return (lambda x, *d: x.permute(*d))(o, 0, 2, 1, 3).reshape(B, S, H * Dh) @ wo

    def _B_ref(x, w1, w2, w3, w4):
        B, S, D = x.shape
        h = _gelu(_layernorm(x, axis=-1) @ w1)
        c = D // 4
        g0 = _pad(h[:, :, 0*c:1*c], ((0, 0), (1, 0), (0, 0)))[:, :S, :]
        g1 = _pad(h[:, :, 1*c:2*c], ((0, 0), (0, 1), (0, 0)))[:, 1:, :]
        g2 = _pad(h[:, :, 2*c:3*c], ((0, 0), (2, 0), (0, 0)))[:, :S, :]
        g3 = _pad(h[:, :, 3*c:   ], ((0, 0), (0, 2), (0, 0)))[:, 2:, :]
        hs = torch.concatenate([g0, g1, g2, g3], axis=-1) @ w2
        x1 = x + hs
        h2 = _gelu(_layernorm(x1, axis=-1) @ w3) @ w4
        return x1 + h2

    x = A_x
    h = x + _A_ref(_layernorm(x, axis=-1), A_wq, A_wk, A_wv, A_wo)
    h2 = h + _B_ref(_layernorm(h, axis=-1), B_w1, B_w2, B_w3, B_w4)
    return h2
```

```cpp
#include <hip/hip_runtime.h>
#include <hip/hip_bf16.h>
#include <stdint.h>

typedef __attribute__((ext_vector_type(8))) __bf16 bf16x8;
typedef __attribute__((ext_vector_type(2))) __bf16 bf16x2;
typedef __attribute__((ext_vector_type(4))) float f32x4;
typedef __attribute__((ext_vector_type(2))) float f32x2;
typedef __attribute__((ext_vector_type(16))) float f32x16;
typedef __attribute__((ext_vector_type(4))) unsigned u32x4;
typedef __attribute__((ext_vector_type(2))) unsigned u32x2;
typedef short v4i16 __attribute__((ext_vector_type(4)));
typedef unsigned short u16;
#define LAS __attribute__((address_space(3)))
#define DI __device__ __forceinline__

constexpr int S = 2048, D = 640, H = 32, DH = 20, F = 1728;
constexpr float LN_EPS = 1e-5f;
constexpr float QSCALE = 0.22360679774997896f * 1.4426950408889634f;
constexpr int KV_TILE = 2624;
constexpr int KV_K1 = 1024, KV_V = 1280;

DI float bf2f(unsigned u) { return __uint_as_float(u << 16); }
DI float bflo(unsigned u) { return __uint_as_float(u << 16); }
DI float bfhi(unsigned u) { return __uint_as_float(u & 0xffff0000u); }
DI unsigned pack2(float a, float b) { f32x2 f = {a, b}; bf16x2 c = __builtin_convertvector(f, bf16x2); return __builtin_bit_cast(unsigned, c); }
DI float rbf(float a) { return bflo(pack2(a, 0.f)); }
DI float gelu_t(float x) {
  const float c0 = -2.f * 0.7978845608028654f * 1.4426950408889634f;
  float u = x * __builtin_fmaf(x * x, 0.044715f, 1.f);
  float e = __builtin_amdgcn_exp2f(u * c0);
  return x * __builtin_amdgcn_rcpf(1.f + e);
}
#define MFMA16(a, b, c) __builtin_amdgcn_mfma_f32_16x16x32_bf16((a), (b), (c), 0, 0, 0)
#define MFMA32(a, b, c) __builtin_amdgcn_mfma_f32_32x32x16_bf16((a), (b), (c), 0, 0, 0)

DI int apack_off(int row, int col, int KS) {
  return (((row >> 4) * KS + (col >> 5)) * 64 + (row & 15) + 16 * ((col >> 3) & 3)) * 8 + (col & 7);
}

DI void prep_task(const u16* __restrict__ W, u16* __restrict__ P, int K, int N, int u) {
  const int no = u % (N / 8), ko = u / (N / 8);
  const u16* src = W + (size_t)(ko * 8) * N + no * 8;
  u32x4 r[8];
#pragma unroll
  for (int i = 0; i < 8; ++i) r[i] = *(const u32x4*)(src + (size_t)i * N);
  const int KS = K / 32;
  const int n0 = no * 8;
  u16* dst = P + ((size_t)((n0 >> 4) * KS + (ko >> 2)) * 64 + (n0 & 15) + 16 * (ko & 3)) * 8;
#pragma unroll
  for (int i = 0; i < 8; ++i) {
    u32x4 o;
#pragma unroll
    for (int p = 0; p < 4; ++p)
      o[p] = __builtin_amdgcn_perm(r[2 * p + 1][i >> 1], r[2 * p][i >> 1], (i & 1) ? 0x07060302u : 0x05040100u);
    *(u32x4*)(dst + i * 8) = o;
  }
}

constexpr int K1_WREG = 30720;

template <bool HASKV>
DI void k1_body(const u16* __restrict__ x, const u16* __restrict__ wq, const u16* __restrict__ wk, const u16* __restrict__ wv,
                u16* __restrict__ Qo, char* __restrict__ KVo, char* lds, float* stl, int mt, int nt) {
  constexpr int NB = HASKV ? 6 : 5;
  const int tid = threadIdx.x, lane = tid & 63;
  const int w = __builtin_amdgcn_readfirstlane(tid >> 6);
  const int g = lane >> 4, r16 = lane & 15;
  char* wreg = lds + w * K1_WREG;
  {
    const int row = (lane & 15) >> 1, half = lane & 1;
#pragma unroll
    for (int i = 0; i < 25; ++i) {
      const int b = 4 * i + g, kb = b / 5, nb = b % 5;
      const u16* src = wq + (size_t)(160 * w + 8 * kb + (row ^ (4 * (kb & 1)))) * D + 80 * nt + 16 * nb + 8 * half;
      __builtin_amdgcn_global_load_lds((const void*)src, (LAS void*)(wreg + i * 1024), 16, 0, 0);
    }
  }
  bf16x8 xa[4][5];
#pragma unroll
  for (int ks = 0; ks < 5; ++ks)
#pragma unroll
    for (int mb = 0; mb < 4; ++mb)
      xa[mb][ks] = *(const bf16x8*)(x + (size_t)(mt * 64 + mb * 16 + r16) * D + 160 * w + 32 * ks + 8 * g);
  if (HASKV) {
#pragma unroll
    for (int i = 0; i < 10; ++i) {
      const int p = i * 64 + lane;
      const int row = p >> 2, c4 = nt * 4 + (p & 3);
      u32x2 v = {0u, 0u};
      if (c4 < 5) v = *(const u32x2*)(wk + (size_t)(160 * w + row) * DH + 4 * c4);
      else if (c4 < 10) v = *(const u32x2*)(wv + (size_t)(160 * w + row) * DH + 4 * (c4 - 5));
      const int kb = row >> 3, rr = (row & 7) ^ (4 * (kb & 1));
      *(u32x2*)(wreg + 25600 + kb * 256 + rr * 32 + (p & 3) * 8) = v;
    }
  }
  float s1[4], s2[4];
  const bf16x2 one2 = __builtin_bit_cast(bf16x2, 0x3F803F80u);
#pragma unroll
  for (int mb = 0; mb < 4; ++mb) {
    float a = 0.f, b = 0.f;
#pragma unroll
    for (int ks = 0; ks < 5; ++ks) {
      const u32x4 u = __builtin_bit_cast(u32x4, xa[mb][ks]);
#pragma unroll
      for (int j = 0; j < 4; ++j) {
        const float e0 = bflo(u[j]), e1 = bfhi(u[j]);
        a += e0 + e1; b += e0 * e0 + e1 * e1;
      }
    }
    a += __shfl_xor(a, 16); b += __shfl_xor(b, 16);
    a += __shfl_xor(a, 32); b += __shfl_xor(b, 32);
    s1[mb] = a; s2[mb] = b;
  }
  if (lane < 16) {
#pragma unroll
    for (int mb = 0; mb < 4; ++mb) { stl[(w * 64 + mb * 16 + lane) * 2] = s1[mb]; stl[(w * 64 + mb * 16 + lane) * 2 + 1] = s2[mb]; }
  }
  f32x4 acc[4][NB], cs[NB];
#pragma unroll
  for (int nb = 0; nb < NB; ++nb) {
    cs[nb] = (f32x4){0.f, 0.f, 0.f, 0.f};
#pragma unroll
    for (int mb = 0; mb < 4; ++mb) acc[mb][nb] = (f32x4){0.f, 0.f, 0.f, 0.f};
  }
  const u32x4 ones4 = {0x3F803F80u, 0x3F803F80u, 0x3F803F80u, 0x3F803F80u};
  const bf16x8 ones = __builtin_bit_cast(bf16x8, ones4);
  asm volatile("s_waitcnt vmcnt(0) lgkmcnt(0)" ::: "memory");
  const int lo_off = (g & 1) * 128 + (r16 >> 2) * 32 + (r16 & 3) * 8, hi_off = lo_off ^ 128;
#pragma unroll
  for (int ks = 0; ks < 5; ++ks) {
    bf16x8 bfr[NB];
#pragma unroll
    for (int nb = 0; nb < NB; ++nb) {
      const char* base = (nb < 5) ? (wreg + ((4 * ks + g) * 5 + nb) * 256) : (wreg + 25600 + (4 * ks + g) * 256);
      v4i16 lo = __builtin_amdgcn_ds_read_tr16_b64_v4i16((LAS v4i16*)(base + lo_off));
      v4i16 hi = __builtin_amdgcn_ds_read_tr16_b64_v4i16((LAS v4i16*)(base + hi_off));
      bfr[nb] = __builtin_bit_cast(bf16x8, __builtin_shufflevector(lo, hi, 0, 1, 2, 3, 4, 5, 6, 7));
    }
#pragma unroll
    for (int nb = 0; nb < NB; ++nb) {
#pragma unroll
      for (int mb = 0; mb < 4; ++mb) acc[mb][nb] = MFMA16(bfr[nb], xa[mb][ks], acc[mb][nb]);
      cs[nb] = MFMA16(bfr[nb], ones, cs[nb]);
    }
  }
  __syncthreads();
  f32x4* xch = (f32x4*)lds;
#pragma unroll
  for (int mb = 0; mb < 4; ++mb) {
    if (mb != w) {
      const int slot = (w < mb) ? w : w - 1;
      f32x4* p = xch + ((mb * 3 + slot) * (2 * NB)) * 64 + lane;
#pragma unroll
      for (int nb = 0; nb < NB; ++nb) { p[nb * 64] = acc[mb][nb]; p[(NB + nb) * 64] = cs[nb]; }
    }
  }
  __syncthreads();
#pragma unroll
  for (int mb = 0; mb < 4; ++mb) {
    if (mb == w) {
      f32x4 a[NB], c[NB];
#pragma unroll
      for (int nb = 0; nb < NB; ++nb) { a[nb] = acc[mb][nb]; c[nb] = cs[nb]; }
#pragma unroll
      for (int sl = 0; sl < 3; ++sl) {
        const f32x4* p = xch + ((mb * 3 + sl) * (2 * NB)) * 64 + lane;
#pragma unroll
        for (int nb = 0; nb < NB; ++nb) { a[nb] += p[nb * 64]; c[nb] += p[(NB + nb) * 64]; }
      }
      float t1 = 0.f, t2 = 0.f;
#pragma unroll
      for (int ww = 0; ww < 4; ++ww) { t1 += stl[(ww * 64 + mb * 16 + r16) * 2]; t2 += stl[(ww * 64 + mb * 16 + r16) * 2 + 1]; }
      const float mean = t1 * (1.f / D);
      const float var = fmaxf(t2 * (1.f / D) - mean * mean, 0.f);
      const float rs = __builtin_amdgcn_rsqf(var + LN_EPS);
      const int tok = mt * 64 + mb * 16 + r16;
#pragma unroll
      for (int nb = 0; nb < 5; ++nb) {
        float z[4];
#pragma unroll
        for (int r = 0; r < 4; ++r) z[r] = (a[nb][r] - mean * c[nb][r]) * (rs * QSCALE);
        u32x2 o = {pack2(z[0], z[1]), pack2(z[2], z[3])};
        *(u32x2*)(Qo + (size_t)tok * D + 80 * nt + 16 * nb + 4 * g) = o;
      }
      if (HASKV) {
        float z[4];
#pragma unroll
        for (int r = 0; r < 4; ++r) z[r] = (a[NB - 1][r] - mean * c[NB - 1][r]) * rs;
        const int c4 = nt * 4 + g;
        char* tb = KVo + (size_t)(tok >> 5) * KV_TILE;
        const int key = tok & 31;
        if (c4 < 4) {
          u32x2 o = {pack2(z[0], z[1]), pack2(z[2], z[3])};
          *(u32x2*)(tb + (key + 32 * (c4 >> 1)) * 16 + (c4 & 1) * 8) = o;
        } else if (c4 == 4) {
          u32x2 o = {pack2(z[0], z[1]), pack2(z[2], z[3])};
          *(u32x2*)(tb + KV_K1 + key * 8) = o;
        } else if (c4 < 10) {
          const int s = key >> 4, hh = (key >> 2) & 1, j = ((key >> 3) & 1) * 4 + (key & 3);
          u16* vb = (u16*)(tb + KV_V) + ((s * 2 + hh) * 21 + 4 * (c4 - 5)) * 8 + j;
#pragma unroll
          for (int r = 0; r < 4; ++r) vb[r * 8] = (u16)(pack2(z[r], 0.f) & 0xffffu);
          if (c4 == 9) vb[4 * 8] = (u16)0x3F80;
        }
      }
    }
  }
}

__global__ __launch_bounds__(256, 1) void k1_kernel(const u16* __restrict__ x, const u16* __restrict__ wq, const u16* __restrict__ wk,
                                                    const u16* __restrict__ wv, const u16* __restrict__ wo, const u16* __restrict__ w1,
                                                    const u16* __restrict__ w2, const u16* __restrict__ w3, const u16* __restrict__ w4,
                                                    u16* __restrict__ Qo, char* __restrict__ KVo, u16* __restrict__ wop,
                                                    u16* __restrict__ w1p, u16* __restrict__ w2p, u16* __restrict__ w3p,
                                                    u16* __restrict__ w4p) {
  __shared__ __attribute__((aligned(16))) char lds[4 * 3 * 12 * 1024 + 2048];
  float* stl = (float*)(lds + 4 * 3 * 12 * 1024);
  {
    const int t = blockIdx.x * 256 + threadIdx.x;
    if (t < 6400) prep_task(wo, wop, D, D, t);
    else if (t < 12800) prep_task(w1, w1p, D, D, t - 6400);
    else if (t < 19200) prep_task(w2, w2p, D, D, t - 12800);
    else if (t < 36480) prep_task(w3, w3p, D, F, t - 19200);
    else if (t < 53760) prep_task(w4, w4p, F, D, t - 36480);
  }
  const int b = blockIdx.x, xc = b & 7, i = b >> 3;
  const int mt = xc * 4 + (i >> 3), nt = i & 7;
  if (nt < 3) k1_body<true>(x, wq, wk, wv, Qo, KVo, lds, stl, mt, nt);
  else k1_body<false>(x, wq, wk, wv, Qo, KVo, lds, stl, mt, nt);
}

constexpr int ACH = 4 * KV_TILE;
constexpr float ATHR = 16.f;

__global__ __launch_bounds__(512, 4) void attn_kernel(const u16* __restrict__ Q, const char* __restrict__ KV, u16* __restrict__ Op) {
  __shared__ __attribute__((aligned(16))) char lds[4 * ACH];
  const int tid = threadIdx.x, lane = tid & 63;
  const int w = __builtin_amdgcn_readfirstlane(tid >> 6);
  const int hi = lane >> 5, l32 = lane & 31;
  const int b = blockIdx.x, xc = b & 7, bi = b >> 3;
  const int tb = xc * 8 + (bi >> 3), hg = bi & 7;
  const int head = hg * 4 + (w & 3), half = w >> 2, wi = w & 3;
  const int tok = tb * 32 + l32;

  auto stage = [&](int c, int buf) {
    const char* src = KV + (size_t)(half * 32 + c * 4) * KV_TILE + lane * 16;
    char* dst = lds + (buf * 2 + half) * ACH;
    __builtin_amdgcn_global_load_lds((const void*)(src + wi * 1024), (LAS void*)(dst + wi * 1024), 16, 0, 0);
    __builtin_amdgcn_global_load_lds((const void*)(src + (wi + 4) * 1024), (LAS void*)(dst + (wi + 4) * 1024), 16, 0, 0);
    if (wi < 2) __builtin_amdgcn_global_load_lds((const void*)(src + (wi + 8) * 1024), (LAS void*)(dst + (wi + 8) * 1024), 16, 0, 0);
    if (wi == 2 && lane < 16) __builtin_amdgcn_global_load_lds((const void*)(src + 10 * 1024), (LAS void*)(dst + 10 * 1024), 16, 0, 0);
  };
  stage(0, 0);
  const u16* qp = Q + (size_t)tok * D + head * DH;
  u32x4 q0u, q1u;
  {
    const u32x2 a = *(const u32x2*)(qp + 8 * hi), c = *(const u32x2*)(qp + 8 * hi + 4);
    q0u = (u32x4){a[0], a[1], c[0], c[1]};
    u32x2 e = *(const u32x2*)(qp + 16);
    q1u = (u32x4){hi ? 0u : e[0], hi ? 0u : e[1], 0u, 0u};
  }
  f32x16 O;
#pragma unroll
  for (int i = 0; i < 16; ++i) O[i] = 0.f;
  float mcur = 0.f;
  const int voff = KV_V + (hi * 21 + (l32 < 20 ? l32 : 20)) * 16;
  __syncthreads();
  for (int c = 0; c < 8; ++c) {
    if (c + 1 < 8) stage(c + 1, (c + 1) & 1);
    const char* cb = lds + ((c & 1) * 2 + half) * ACH;
#pragma unroll
    for (int t = 0; t < 4; ++t) {
      const char* tp = cb + t * KV_TILE;
      const bf16x8 k0 = *(const bf16x8*)(tp + lane * 16);
      const u32x2 k1d = *(const u32x2*)(tp + KV_K1 + l32 * 8);
      const bf16x8 v0 = *(const bf16x8*)(tp + voff);
      const bf16x8 v1 = *(const bf16x8*)(tp + voff + 2 * 21 * 16);
      const u32x4 k1u = {k1d[0], k1d[1], 0x00003F80u, 0u};
      f32x16 Sx;
#pragma unroll
      for (int i = 0; i < 16; ++i) Sx[i] = 0.f;
      Sx = MFMA32(k0, __builtin_bit_cast(bf16x8, q0u), Sx);
      Sx = MFMA32(__builtin_bit_cast(bf16x8, k1u), __builtin_bit_cast(bf16x8, q1u), Sx);
      float mx = fmaxf(fmaxf(Sx[0], Sx[1]), Sx[2]);
#pragma unroll
      for (int i = 3; i < 15; i += 2) mx = fmaxf(fmaxf(mx, Sx[i]), Sx[i + 1]);
      mx = fmaxf(mx, Sx[15]);
      const bool first = (c == 0 && t == 0);
      if (first || __any(mx > ATHR)) {
        const float mo = fmaxf(mx, __shfl_xor(mx, 32));
        const float dd = first ? mo : fmaxf(mo, 0.f);
        const float mnew = rbf(mcur + dd);
        const float delta = mnew - mcur;
        const float sc = first ? 0.f : __builtin_amdgcn_exp2f(-delta);
#pragma unroll
        for (int i = 0; i < 16; ++i) { Sx[i] -= delta; O[i] *= sc; }
        mcur = mnew;
        q1u[2] = hi ? 0u : (pack2(-mnew, 0.f) & 0xffffu);
      }
      u32x4 p0, p1;
#pragma unroll
      for (int i = 0; i < 4; ++i) {
        p0[i] = pack2(__builtin_amdgcn_exp2f(Sx[2 * i]), __builtin_amdgcn_exp2f(Sx[2 * i + 1]));
        p1[i] = pack2(__builtin_amdgcn_exp2f(Sx[8 + 2 * i]), __builtin_amdgcn_exp2f(Sx[8 + 2 * i + 1]));
      }
      O = MFMA32(v0, __builtin_bit_cast(bf16x8, p0), O);
      O = MFMA32(v1, __builtin_bit_cast(bf16x8, p1), O);
    }
    __syncthreads();
  }
  float* cx = (float*)lds;
  if (half == 1) {
#pragma unroll
    for (int i = 0; i < 16; ++i) cx[(wi * 17 + i) * 64 + lane] = O[i];
    cx[(wi * 17 + 16) * 64 + lane] = mcur;
  }
  __syncthreads();
  if (half == 0) {
    const float m2 = cx[(wi * 17 + 16) * 64 + lane];
    const float mm = fmaxf(mcur, m2);
    const float f1 = __builtin_amdgcn_exp2f(mcur - mm), f2 = __builtin_amdgcn_exp2f(m2 - mm);
#pragma unroll
    for (int i = 0; i < 16; ++i) O[i] = O[i] * f1 + cx[(wi * 17 + i) * 64 + lane] * f2;
    const float l = hi ? O[8] : O[12];
    const float rl = 1.f / l;
    const int col0 = head * DH + 4 * hi;
#pragma unroll
    for (int q = 0; q < 3; ++q) {
      if (q < 2 || hi == 0) {
        u32x2 o = {pack2(O[4 * q] * rl, O[4 * q + 1] * rl), pack2(O[4 * q + 2] * rl, O[4 * q + 3] * rl)};
        *(u32x2*)(Op + apack_off(tok, col0 + 8 * q, 20)) = o;
      }
    }
  }
}

enum { EPI_WO = 0, EPI_W1 = 1, EPI_W2 = 2, EPI_W3 = 3, EPI_W4 = 4 };

template <int EPI>
__global__ __launch_bounds__(256, 1) void gemm_kernel(const u16* __restrict__ A, const u16* __restrict__ Bp, const u16* __restrict__ r1,
                                                      const u16* __restrict__ r2, const float* __restrict__ stin,
                                                      float* __restrict__ stout, u16* __restrict__ out) {
  constexpr bool LN = (EPI == EPI_W1 || EPI == EPI_W3);
  constexpr bool NEEDST = (EPI == EPI_W1 || EPI == EPI_W2 || EPI == EPI_W3);
  constexpr int MB = 4;
  constexpr int NB = (EPI == EPI_W3) ? 7 : 5;
  constexpr int KW = (EPI == EPI_W3) ? 2 : 4;
  constexpr int KST = (EPI == EPI_W4) ? 54 : 20;
  constexpr int KSW = (EPI == EPI_W4) ? 14 : (EPI == EPI_W3 ? 10 : 5);
  constexpr int DEPTH = (EPI == EPI_W4) ? 6 : 5;
  constexpr int OWN = MB / KW;
  constexpr int MSG = OWN * NB + (LN ? NB : 0);
  __shared__ __attribute__((aligned(16))) f32x4 xch[4 * (KW - 1) * MSG * 64];

  const int tid = threadIdx.x, lane = tid & 63;
  const int w = __builtin_amdgcn_readfirstlane(tid >> 6);
  const int g = lane >> 4, r16 = lane & 15;
  const int kw = w % KW, mw = w / KW;
  const int b = blockIdx.x, xc = b & 7, bi = b >> 3;
  int mblk0, nblk0, nbcnt, nt;
  if (EPI == EPI_W3) {
    const int mt = xc * 2 + (bi >> 4); nt = bi & 15;
    mblk0 = mt * 8 + mw * 4;
    nblk0 = nt < 12 ? 7 * nt : 84 + 6 * (nt - 12);
    nbcnt = nt < 12 ? 7 : 6;
  } else {
    const int mt = xc * 4 + (bi >> 3); nt = bi & 7;
    mblk0 = mt * 4; nblk0 = nt * 5; nbcnt = 5;
  }
  int ks0, kcnt;
  if (EPI == EPI_W4) { ks0 = kw < 2 ? 14 * kw : 28 + 13 * (kw - 2); kcnt = kw < 2 ? 14 : 13; }
  else { ks0 = kw * KSW; kcnt = KSW; }

  const u16* Ab = A + ((size_t)mblk0 * KST + ks0) * 512 + lane * 8;
  const u16* Bb = Bp + ((size_t)nblk0 * KST + ks0) * 512 + lane * 8;
  bf16x8 af[DEPTH][MB], bfr[DEPTH][NB];
  auto load_ks = [&](int ks, int slot) {
    const int kk = (EPI == EPI_W4) ? (ks < kcnt ? ks : kcnt - 1) : ks;
#pragma unroll
    for (int mb = 0; mb < MB; ++mb) af[slot][mb] = *(const bf16x8*)(Ab + ((size_t)mb * KST + kk) * 512);
#pragma unroll
    for (int nb = 0; nb < NB; ++nb) {
      const int nn = (EPI == EPI_W3) ? (nb < nbcnt ? nb : nbcnt - 1) : nb;
      bfr[slot][nb] = *(const bf16x8*)(Bb + ((size_t)nn * KST + kk) * 512);
    }
  };
#pragma unroll
  for (int d = 0; d < DEPTH; ++d)
    if (d < KSW) load_ks(d, d);

  f32x4 stv[OWN][4];
  u32x2 ra[OWN][NB], rb[OWN][NB];
#pragma unroll
  for (int o = 0; o < OWN; ++o) {
    const int tok = (mblk0 + kw * OWN + o) * 16 + r16;
    if (NEEDST) {
#pragma unroll
      for (int q = 0; q < 4; ++q) stv[o][q] = *(const f32x4*)(stin + (size_t)tok * 16 + q * 4);
    }
#pragma unroll
    for (int nb = 0; nb < NB; ++nb) {
      const int col = (nblk0 + nb) * 16 + 4 * g;
      if (EPI == EPI_WO) ra[o][nb] = *(const u32x2*)(r1 + (size_t)tok * D + col);
      if (EPI == EPI_W2 || EPI == EPI_W4) ra[o][nb] = *(const u32x2*)(r1 + apack_off(tok, col, 20));
      if (EPI == EPI_W4) rb[o][nb] = *(const u32x2*)(r2 + apack_off(tok, col, 20));
    }
  }

  f32x4 acc[MB][NB], cs[NB];
#pragma unroll
  for (int nb = 0; nb < NB; ++nb) {
    cs[nb] = (f32x4){0.f, 0.f, 0.f, 0.f};
#pragma unroll
    for (int mb = 0; mb < MB; ++mb) acc[mb][nb] = (f32x4){0.f, 0.f, 0.f, 0.f};
  }
  const u32x4 ones4 = {0x3F803F80u, 0x3F803F80u, 0x3F803F80u, 0x3F803F80u};
  const bf16x8 ones = __builtin_bit_cast(bf16x8, ones4);
#pragma unroll
  for (int ks = 0; ks < KSW; ++ks) {
    const int slot = ks % DEPTH;
    if (EPI != EPI_W4 || ks < 13 || ks < kcnt) {
#pragma unroll
      for (int nb = 0; nb < NB; ++nb) {
#pragma unroll
        for (int mb = 0; mb < MB; ++mb) acc[mb][nb] = MFMA16(bfr[slot][nb], af[slot][mb], acc[mb][nb]);
        if (LN) cs[nb] = MFMA16(bfr[slot][nb], ones, cs[nb]);
      }
    }
    if (ks + DEPTH < KSW) load_ks(ks + DEPTH, slot);
  }

#pragma unroll
  for (int dk = 0; dk < KW; ++dk) {
    if (dk != kw) {
      const int slot = (kw < dk) ? kw : kw - 1;
      f32x4* p = xch + (((mw * KW + dk) * (KW - 1) + slot) * MSG) * 64 + lane;
#pragma unroll
      for (int o = 0; o < OWN; ++o)
#pragma unroll
        for (int nb = 0; nb < NB; ++nb) p[(o * NB + nb) * 64] = acc[dk * OWN + o][nb];
      if (LN) {
#pragma unroll
        for (int nb = 0; nb < NB; ++nb) p[(OWN * NB + nb) * 64] = cs[nb];
      }
    }
  }
  __syncthreads();
#pragma unroll
  for (int dk = 0; dk < KW; ++dk) {
    if (dk == kw) {
      f32x4 a[OWN][NB], c[NB];
#pragma unroll
      for (int nb = 0; nb < NB; ++nb) {
        c[nb] = cs[nb];
#pragma unroll
        for (int o = 0; o < OWN; ++o) a[o][nb] = acc[dk * OWN + o][nb];
      }
#pragma unroll
      for (int sl = 0; sl < KW - 1; ++sl) {
        const f32x4* p = xch + (((mw * KW + dk) * (KW - 1) + sl) * MSG) * 64 + lane;
#pragma unroll
        for (int nb = 0; nb < NB; ++nb) {
#pragma unroll
          for (int o = 0; o < OWN; ++o) a[o][nb] += p[(o * NB + nb) * 64];
          if (LN) c[nb] += p[(OWN * NB + nb) * 64];
        }
      }
#pragma unroll
      for (int o = 0; o < OWN; ++o) {
        const int tok = (mblk0 + dk * OWN + o) * 16 + r16;
        float mean = 0.f, rs = 0.f;
        if (NEEDST) {
          float t1 = 0.f, t2 = 0.f;
#pragma unroll
          for (int q = 0; q < 4; ++q) { t1 += stv[o][q][0] + stv[o][q][2]; t2 += stv[o][q][1] + stv[o][q][3]; }
          mean = t1 * (1.f / D);
          const float var = fmaxf(t2 * (1.f / D) - mean * mean, 0.f);
          rs = __builtin_amdgcn_rsqf(var + LN_EPS);
          if (EPI == EPI_W1) rs = rs * __builtin_amdgcn_rsqf(var * rs * rs + LN_EPS);
        }
        float q1 = 0.f, q2 = 0.f;
#pragma unroll
        for (int nb = 0; nb < NB; ++nb) {
          const int col = (nblk0 + nb) * 16 + 4 * g;
          float z[4];
          if (EPI == EPI_WO) {
            const float xr[4] = {bflo(ra[o][nb][0]), bfhi(ra[o][nb][0]), bflo(ra[o][nb][1]), bfhi(ra[o][nb][1])};
#pragma unroll
            for (int r = 0; r < 4; ++r) { z[r] = rbf(xr[r] + rbf(a[o][nb][r])); q1 += z[r]; q2 += z[r] * z[r]; }
            u32x2 ov = {pack2(z[0], z[1]), pack2(z[2], z[3])};
            *(u32x2*)(out + apack_off(tok, col, 20)) = ov;
          } else if (EPI == EPI_W1) {
#pragma unroll
            for (int r = 0; r < 4; ++r) z[r] = gelu_t(rbf((a[o][nb][r] - mean * c[nb][r]) * rs));
            u32x2 ov = {pack2(z[0], z[1]), pack2(z[2], z[3])};
            const int grp = nt >> 1;
            const int sh = (grp == 0) ? 1 : (grp == 1) ? -1 : (grp == 2) ? 2 : -2;
            const int t2 = tok + sh;
            if (t2 >= 0 && t2 < S) *(u32x2*)(out + apack_off(t2, col, 20)) = ov;
            if (tok - sh < 0 || tok - sh >= S) *(u32x2*)(out + apack_off(tok, col, 20)) = (u32x2){0u, 0u};
          } else if (EPI == EPI_W2) {
            const float hr[4] = {bflo(ra[o][nb][0]), bfhi(ra[o][nb][0]), bflo(ra[o][nb][1]), bfhi(ra[o][nb][1])};
#pragma unroll
            for (int r = 0; r < 4; ++r) { z[r] = rbf(rbf((hr[r] - mean) * rs) + rbf(a[o][nb][r])); q1 += z[r]; q2 += z[r] * z[r]; }
            u32x2 ov = {pack2(z[0], z[1]), pack2(z[2], z[3])};
            *(u32x2*)(out + apack_off(tok, col, 20)) = ov;
          } else if (EPI == EPI_W3) {
#pragma unroll
            for (int r = 0; r < 4; ++r) z[r] = gelu_t(rbf((a[o][nb][r] - mean * c[nb][r]) * rs));
            u32x2 ov = {pack2(z[0], z[1]), pack2(z[2], z[3])};
            if (nb < nbcnt) *(u32x2*)(out + apack_off(tok, col, 54)) = ov;
          } else {
            const float hr[4] = {bflo(ra[o][nb][0]), bfhi(ra[o][nb][0]), bflo(ra[o][nb][1]), bfhi(ra[o][nb][1])};
            const float xr[4] = {bflo(rb[o][nb][0]), bfhi(rb[o][nb][0]), bflo(rb[o][nb][1]), bfhi(rb[o][nb][1])};
#pragma unroll
            for (int r = 0; r < 4; ++r) z[r] = hr[r] + rbf(xr[r] + rbf(a[o][nb][r]));
            u32x2 ov = {pack2(z[0], z[1]), pack2(z[2], z[3])};
            *(u32x2*)(out + (size_t)tok * D + col) = ov;
          }
        }
        if (EPI == EPI_WO || EPI == EPI_W2) {
          q1 += __shfl_xor(q1, 16); q2 += __shfl_xor(q2, 16);
          q1 += __shfl_xor(q1, 32); q2 += __shfl_xor(q2, 32);
          if (lane < 16) *(f32x2*)(stout + (size_t)tok * 16 + nt * 2) = (f32x2){q1, q2};
        }
      }
    }
  }
}

extern "C" void kernel_launch(void* const* d_in, const int* in_sizes, int n_in, void* d_out, int out_size, void* d_ws,
                              size_t ws_size, hipStream_t stream) {
  const u16* x = (const u16*)d_in[0];
  const u16* wq = (const u16*)d_in[1];
  const u16* wk = (const u16*)d_in[2];
  const u16* wv = (const u16*)d_in[3];
  const u16* wo = (const u16*)d_in[4];
  const u16* w1 = (const u16*)d_in[6];
  const u16* w2 = (const u16*)d_in[7];
  const u16* w3 = (const u16*)d_in[8];
  const u16* w4 = (const u16*)d_in[9];
  u16* out = (u16*)d_out;
  char* ws = (char*)d_ws;
  size_t off = 0;
  auto take = [&](size_t bytes) { char* p = ws + off; off += (bytes + 4095) & ~(size_t)4095; return p; };
  u16* Qb = (u16*)take((size_t)S * D * 2);
  char* KVb = take((size_t)64 * KV_TILE + 1024);
  u16* Ob = (u16*)take((size_t)S * D * 2);
  u16* hb = (u16*)take((size_t)S * D * 2);
  u16* hhb = (u16*)take((size_t)S * D * 2);
  u16* x1b = (u16*)take((size_t)S * D * 2);
  u16* gb = (u16*)take((size_t)S * F * 2);
  u16* wop = (u16*)take((size_t)D * D * 2);
  u16* w1p = (u16*)take((size_t)D * D * 2);
  u16* w2p = (u16*)take((size_t)D * D * 2);
  u16* w3p = (u16*)take((size_t)D * F * 2);
  u16* w4p = (u16*)take((size_t)D * F * 2);
  float* sth = (float*)take((size_t)S * 16 * 4);
  float* stx = (float*)take((size_t)S * 16 * 4);

  k1_kernel<<<256, 256, 0, stream>>>(x, wq, wk, wv, wo, w1, w2, w3, w4, Qb, KVb, wop, w1p, w2p, w3p, w4p);
  attn_kernel<<<512, 512, 0, stream>>>(Qb, KVb, Ob);
  gemm_kernel<EPI_WO><<<256, 256, 0, stream>>>(Ob, wop, x, nullptr, nullptr, sth, hb);
  gemm_kernel<EPI_W1><<<256, 256, 0, stream>>>(hb, w1p, nullptr, nullptr, sth, nullptr, hhb);
  gemm_kernel<EPI_W2><<<256, 256, 0, stream>>>(hhb, w2p, hb, nullptr, sth, stx, x1b);
  gemm_kernel<EPI_W3><<<256, 256, 0, stream>>>(x1b, w3p, nullptr, nullptr, stx, nullptr, gb);
  gemm_kernel<EPI_W4><<<256, 256, 0, stream>>>(gb, w4p, hb, x1b, nullptr, nullptr, out);
}
```

```cpp
#include <hip/hip_runtime.h>
#include <hip/hip_bf16.h>
#include <stdint.h>

typedef __attribute__((ext_vector_type(8))) __bf16 bf16x8;
typedef __attribute__((ext_vector_type(2))) __bf16 bf16x2;
typedef __attribute__((ext_vector_type(4))) float f32x4;
typedef __attribute__((ext_vector_type(2))) float f32x2;
typedef __attribute__((ext_vector_type(16))) float f32x16;
typedef __attribute__((ext_vector_type(4))) unsigned u32x4;
typedef __attribute__((ext_vector_type(2))) unsigned u32x2;
typedef short v4i16 __attribute__((ext_vector_type(4)));
typedef unsigned short u16;
#define LAS __attribute__((address_space(3)))
#define DI __device__ __forceinline__

constexpr int S = 2048, D = 640, H = 32, DH = 20, F = 1728;
constexpr float LN_EPS = 1e-5f;
constexpr float QSCALE = 0.22360679774997896f * 1.4426950408889634f;
constexpr int KV_TILE = 2624;
constexpr int KV_K1 = 1024, KV_V = 1280;

DI float bf2f(unsigned u) { return __uint_as_float(u << 16); }
DI float bflo(unsigned u) { return __uint_as_float(u << 16); }
DI float bfhi(unsigned u) { return __uint_as_float(u & 0xffff0000u); }
DI unsigned pack2(float a, float b) { f32x2 f = {a, b}; bf16x2 c = __builtin_convertvector(f, bf16x2); return __builtin_bit_cast(unsigned, c); }
DI float rbf(float a) { return bflo(pack2(a, 0.f)); }
DI float gelu_t(float x) {
  const float c0 = -2.f * 0.7978845608028654f * 1.4426950408889634f;
  float u = x * __builtin_fmaf(x * x, 0.044715f, 1.f);
  float e = __builtin_amdgcn_exp2f(u * c0);
  return x * __builtin_amdgcn_rcpf(1.f + e);
}
#define MFMA16(a, b, c) __builtin_amdgcn_mfma_f32_16x16x32_bf16((a), (b), (c), 0, 0, 0)
#define MFMA32(a, b, c) __builtin_amdgcn_mfma_f32_32x32x16_bf16((a), (b), (c), 0, 0, 0)

DI void glds16(const void* g, unsigned lds_base) {
  unsigned sv;
  asm volatile("s_mov_b32 %0, m0\n\ts_mov_b32 m0, %2\n\ts_nop 0\n\tglobal_load_lds_dwordx4 %1, off\n\ts_mov_b32 m0, %0"
               : "=&s"(sv) : "v"(g), "s"(lds_base) : "memory");
}
DI unsigned lds_addr(const void* p) { return (unsigned)(size_t)(const LAS char*)p; }

DI int apack_off(int row, int col, int KS) {
  return (((row >> 4) * KS + (col >> 5)) * 64 + (row & 15) + 16 * ((col >> 3) & 3)) * 8 + (col & 7);
}

constexpr int PW_X = 512;
constexpr int PW_A = PW_X + 400;
constexpr int PW_B = PW_A + 280;
constexpr int PW_C = PW_B + 270;
constexpr int PW_D = PW_C + 20;

__global__ __launch_bounds__(256) void pack_kernel(const u16* __restrict__ x, const u16* __restrict__ wq, const u16* __restrict__ wk,
                                                   const u16* __restrict__ wv, const u16* __restrict__ wo, const u16* __restrict__ w1,
                                                   const u16* __restrict__ w2, const u16* __restrict__ w3, const u16* __restrict__ w4,
                                                   u16* __restrict__ xp, float* __restrict__ stx, u16* __restrict__ wqp,
                                                   u16* __restrict__ wop, u16* __restrict__ w1p, u16* __restrict__ w2p,
                                                   u16* __restrict__ w3p, u16* __restrict__ w4p) {
  __shared__ __attribute__((aligned(16))) char lds[4 * 8 * 1152];
  const int lane = threadIdx.x & 63;
  const int wv_ = __builtin_amdgcn_readfirstlane(blockIdx.x * 4 + (threadIdx.x >> 6));
  if (wv_ < PW_X) {
    const int mb = wv_ >> 2, kq = wv_ & 3, r16 = lane & 15, g = lane >> 4;
    u32x4 v[5];
#pragma unroll
    for (int i = 0; i < 5; ++i) v[i] = *(const u32x4*)(x + (size_t)(mb * 16 + r16) * D + (kq * 5 + i) * 32 + g * 8);
    float s1 = 0.f, s2 = 0.f;
#pragma unroll
    for (int i = 0; i < 5; ++i) {
      *(u32x4*)(xp + ((size_t)(mb * 20 + kq * 5 + i) * 64 + lane) * 8) = v[i];
#pragma unroll
      for (int j = 0; j < 4; ++j) { const float e0 = bflo(v[i][j]), e1 = bfhi(v[i][j]); s1 += e0 + e1; s2 += e0 * e0 + e1 * e1; }
    }
    s1 += __shfl_xor(s1, 16); s2 += __shfl_xor(s2, 16);
    s1 += __shfl_xor(s1, 32); s2 += __shfl_xor(s2, 32);
    if (lane < 32) *(f32x2*)(stx + (size_t)(mb * 16 + r16) * 16 + (kq + 4 * g) * 2) = (g == 0) ? (f32x2){s1, s2} : (f32x2){0.f, 0.f};
    return;
  }
  if (wv_ >= PW_D) return;
  const int blk = lane >> 3, no2 = lane & 1, kg = (lane >> 1) & 3;
  const u16* W; u16* P; int N, KS, ks, nb0, nblk = 8;
  bool kvt = false;
  if (wv_ < PW_A) {
    const int u = wv_ - PW_X, which = u / 100, uu = u % 100;
    W = which == 0 ? wq : which == 1 ? wo : which == 2 ? w1 : w2;
    P = which == 0 ? wqp : which == 1 ? wop : which == 2 ? w1p : w2p;
    N = D; KS = 20; ks = uu / 5; nb0 = (uu % 5) * 8;
  } else if (wv_ < PW_B) {
    const int u = wv_ - PW_A; W = w3; P = w3p; N = F; KS = 20; ks = u / 14; nb0 = (u % 14) * 8; if (nb0 == 104) nblk = 4;
  } else if (wv_ < PW_C) {
    const int u = wv_ - PW_B; W = w4; P = w4p; N = D; KS = 54; ks = u / 5; nb0 = (u % 5) * 8;
  } else {
    W = wk; P = wqp; N = DH; KS = 20; ks = wv_ - PW_C; nb0 = 40; nblk = 3; kvt = true;
  }
  u32x4 r[8];
  if (!kvt) {
    const int bb = blk < nblk ? blk : 0;
    const u16* src = W + (size_t)(ks * 32 + kg * 8) * N + (nb0 + bb) * 16 + no2 * 8;
#pragma unroll
    for (int i = 0; i < 8; ++i) r[i] = *(const u32x4*)(src + (size_t)i * N);
  } else {
    const u16* sp[2]; unsigned msk[2];
#pragma unroll
    for (int h2 = 0; h2 < 2; ++h2) {
      const int c4 = blk * 4 + no2 * 2 + h2;
      sp[h2] = (c4 < 5 ? wk + 4 * c4 : wv + 4 * (c4 < 10 ? c4 - 5 : 0)) + (size_t)(ks * 32 + kg * 8) * DH;
      msk[h2] = c4 < 10 ? 0xffffffffu : 0u;
    }
#pragma unroll
    for (int i = 0; i < 8; ++i) {
#pragma unroll
      for (int h2 = 0; h2 < 2; ++h2) {
        const u32x2 e = *(const u32x2*)(sp[h2] + i * DH);
        r[i][2 * h2] = e[0] & msk[h2]; r[i][2 * h2 + 1] = e[1] & msk[h2];
      }
    }
  }
  char* wl = lds + (threadIdx.x >> 6) * (8 * 1152);
  {
    char* d = wl + blk * 1152 + (no2 + 2 * kg) * 144;
#pragma unroll
    for (int i = 0; i < 8; ++i) {
      u32x4 o;
#pragma unroll
      for (int p = 0; p < 4; ++p)
        o[p] = __builtin_amdgcn_perm(r[2 * p + 1][i >> 1], r[2 * p][i >> 1], (i & 1) ? 0x07060302u : 0x05040100u);
      *(u32x4*)(d + i * 16) = o;
    }
  }
  asm volatile("s_waitcnt lgkmcnt(0)" ::: "memory");
#pragma unroll
  for (int bq = 0; bq < 8; ++bq) {
    if (bq < nblk) {
      const u32x4 o = *(const u32x4*)(wl + bq * 1152 + (lane >> 3) * 144 + (lane & 7) * 16);
      *(u32x4*)(P + ((size_t)((nb0 + bq) * KS + ks) * 64 + lane) * 8) = o;
    }
  }
}

constexpr int ACH = 4 * KV_TILE;
constexpr float ATHR = 16.f;
#define SB() __builtin_amdgcn_sched_barrier(0)
#define EXP2(x) __builtin_amdgcn_exp2f(x)

template <bool ONLINE>
DI void attn_pass(const char* __restrict__ KV, char* lds, int lane, int wi, int half, const u32x4 q0u, u32x4 q1u, f32x16& O,
                  float& mcur) {
  const int hi = lane >> 5, l32 = lane & 31;
  const unsigned lds0 = lds_addr(lds);
  auto stage = [&](int c, int buf) {
    const char* src = KV + (size_t)(half * 32 + c * 4) * KV_TILE + lane * 16;
    const unsigned dst = __builtin_amdgcn_readfirstlane(lds0 + (buf * 2 + half) * ACH + wi * 1024);
    glds16(src + wi * 1024, dst);
    glds16(src + (wi + 4) * 1024, dst + 4096);
    if (wi < 2) glds16(src + (wi + 8) * 1024, dst + 8192);
    if (wi == 2 && lane < 16) glds16(src + 10 * 1024, dst + 8192);
  };
  stage(0, 0); stage(1, 1);
  f32x16 Z;
#pragma unroll
  for (int i = 0; i < 16; ++i) { O[i] = 0.f; Z[i] = 0.f; }
  const int koff = lane * 16, k1off = KV_K1 + l32 * 8;
  const int voff = KV_V + (hi * 21 + (l32 < 20 ? l32 : 20)) * 16;
  asm volatile("s_waitcnt vmcnt(0)" ::: "memory");
  __syncthreads();
  const char* hb = lds + half * ACH;
  f32x16 Sx;
  bf16x8 k0n, v0, v1; u32x2 k1n;
  {
    const bf16x8 k0 = *(const bf16x8*)(hb + koff);
    const u32x2 k1d = *(const u32x2*)(hb + k1off);
    const u32x4 k1u = {k1d[0], k1d[1], 0x00003F80u, 0u};
    Sx = MFMA32(k0, __builtin_bit_cast(bf16x8, q0u), Z);
    Sx = MFMA32(__builtin_bit_cast(bf16x8, k1u), __builtin_bit_cast(bf16x8, q1u), Sx);
    k0n = *(const bf16x8*)(hb + KV_TILE + koff); k1n = *(const u32x2*)(hb + KV_TILE + k1off);
    v0 = *(const bf16x8*)(hb + voff); v1 = *(const bf16x8*)(hb + voff + 672);
    if (ONLINE) {
      float mx = fmaxf(fmaxf(Sx[0], Sx[1]), Sx[2]);
#pragma unroll
      for (int i = 3; i < 15; i += 2) mx = fmaxf(fmaxf(mx, Sx[i]), Sx[i + 1]);
      mx = fmaxf(mx, Sx[15]);
      const float mo = fmaxf(mx, __shfl_xor(mx, 32));
      mcur = rbf(mo);
#pragma unroll
      for (int i = 0; i < 16; ++i) Sx[i] -= mcur;
      q1u[2] = hi ? 0u : (pack2(-mcur, 0.f) & 0xffffu);
    }
  }
  int bcur = 0;
  for (int c = 0; c < 8; ++c) {
    const int bn1 = (bcur == 2) ? 0 : bcur + 1, bn2 = (bn1 == 2) ? 0 : bn1 + 1;
    if (c + 2 < 8) stage(c + 2, bn2);
    const char* cb = hb + bcur * (2 * ACH);
    const char* nb = hb + bn1 * (2 * ACH);
#pragma unroll
    for (int t = 0; t < 4; ++t) {
      const char* tv = (t < 3) ? cb + (t + 1) * KV_TILE : nb;
      const char* tk = (t < 2) ? cb + (t + 2) * KV_TILE : nb + (t - 2) * KV_TILE;
      u32x4 p0, p1;
      f32x16 Sn;
      SB();
      const u32x4 k1u = {k1n[0], k1n[1], 0x00003F80u, 0u};
      Sn = MFMA32(k0n, __builtin_bit_cast(bf16x8, q0u), Z);
      SB();
      p0[0] = pack2(EXP2(Sx[0]), EXP2(Sx[1])); p0[1] = pack2(EXP2(Sx[2]), EXP2(Sx[3]));
      SB();
      Sn = MFMA32(__builtin_bit_cast(bf16x8, k1u), __builtin_bit_cast(bf16x8, q1u), Sn);
      SB();
      p0[2] = pack2(EXP2(Sx[4]), EXP2(Sx[5])); p0[3] = pack2(EXP2(Sx[6]), EXP2(Sx[7]));
      SB();
      O = MFMA32(v0, __builtin_bit_cast(bf16x8, p0), O);
      k0n = *(const bf16x8*)(tk + koff); k1n = *(const u32x2*)(tk + k1off);
      v0 = *(const bf16x8*)(tv + voff);
      SB();
      p1[0] = pack2(EXP2(Sx[8]), EXP2(Sx[9])); p1[1] = pack2(EXP2(Sx[10]), EXP2(Sx[11]));
      p1[2] = pack2(EXP2(Sx[12]), EXP2(Sx[13])); p1[3] = pack2(EXP2(Sx[14]), EXP2(Sx[15]));
      SB();
      O = MFMA32(v1, __builtin_bit_cast(bf16x8, p1), O);
      v1 = *(const bf16x8*)(tv + voff + 672);
      SB();
      if (ONLINE) {
        float mx = fmaxf(fmaxf(Sn[0], Sn[1]), Sn[2]);
#pragma unroll
        for (int i = 3; i < 15; i += 2) mx = fmaxf(fmaxf(mx, Sn[i]), Sn[i + 1]);
        mx = fmaxf(mx, Sn[15]);
        if (!(c == 7 && t == 3) && __any(mx > ATHR)) {
          const float mo = fmaxf(mx, __shfl_xor(mx, 32));
          const float mnew = rbf(mcur + fmaxf(mo, 0.f));
          const float delta = mnew - mcur;
          const float sc = EXP2(-delta);
#pragma unroll
          for (int i = 0; i < 16; ++i) { Sn[i] -= delta; O[i] *= sc; }
          mcur = mnew;
          q1u[2] = hi ? 0u : (pack2(-mnew, 0.f) & 0xffffu);
        }
      }
      Sx = Sn;
    }
    asm volatile("s_waitcnt vmcnt(0)" ::: "memory");
    __syncthreads();
    bcur = bn1;
  }
}

__global__ __launch_bounds__(512, 4) void attn_kernel(const u16* __restrict__ Q, const char* __restrict__ KV,
                                                      const float* __restrict__ kmax, u16* __restrict__ Op) {
  __shared__ __attribute__((aligned(16))) char lds[6 * ACH];
  const int tid = threadIdx.x, lane = tid & 63;
  const int w = __builtin_amdgcn_readfirstlane(tid >> 6);
  const int hi = lane >> 5, l32 = lane & 31;
  const int b = blockIdx.x, xc = b & 7, bi = b >> 3;
  const int tb = xc * 8 + (bi >> 3), hg = bi & 7;
  const int head = hg * 4 + (w & 3), half = w >> 2, wi = w & 3;
  const int tok = tb * 32 + l32;
  const u16* qp = Q + (size_t)tok * D + head * DH;
  u32x4 q0u, q1u;
  {
    const u32x2 a = *(const u32x2*)(qp + 8 * hi), c = *(const u32x2*)(qp + 8 * hi + 4);
    q0u = (u32x4){a[0], a[1], c[0], c[1]};
    u32x2 e = *(const u32x2*)(qp + 16);
    q1u = (u32x4){hi ? 0u : e[0], hi ? 0u : e[1], 0u, 0u};
  }
  float mcur;
  {
    const f32x2 ka = *(const f32x2*)(kmax + lane * 2), kb = *(const f32x2*)(kmax + 128 + lane * 2);
    float ma = fmaxf(ka[0], ka[1]), mb = fmaxf(kb[0], kb[1]);
#pragma unroll
    for (int o = 1; o < 64; o <<= 1) { ma = fmaxf(ma, __shfl_xor(ma, o)); mb = fmaxf(mb, __shfl_xor(mb, o)); }
    float qq = 0.f;
#pragma unroll
    for (int i = 0; i < 4; ++i) { qq += bflo(q0u[i]) * bflo(q0u[i]) + bfhi(q0u[i]) * bfhi(q0u[i]); }
#pragma unroll
    for (int i = 0; i < 2; ++i) { qq += bflo(q1u[i]) * bflo(q1u[i]) + bfhi(q1u[i]) * bfhi(q1u[i]); }
    qq += __shfl_xor(qq, 32);
    mcur = rbf(__builtin_sqrtf(qq * (ma + mb)) * 1.02f + 1e-30f);
    q1u[2] = hi ? 0u : (pack2(-mcur, 0.f) & 0xffffu);
  }
  f32x16 O;
  attn_pass<false>(KV, lds, lane, wi, half, q0u, q1u, O, mcur);
  {
    const float l = hi ? O[8] : O[12];
    const int bad = !(l >= 8.6736174e-19f && l < 1e30f);
    if (__syncthreads_or(bad)) {
      q1u[2] = 0u;
      attn_pass<true>(KV, lds, lane, wi, half, q0u, q1u, O, mcur);
    }
  }
  float* cx = (float*)lds;
  if (half == 1) {
#pragma unroll
    for (int i = 0; i < 16; ++i) cx[(wi * 17 + i) * 64 + lane] = O[i];
    cx[(wi * 17 + 16) * 64 + lane] = mcur;
  }
  __syncthreads();
  if (half == 0) {
    const float m2 = cx[(wi * 17 + 16) * 64 + lane];
    const float mm = fmaxf(mcur, m2);
    const float f1 = EXP2(mcur - mm), f2 = EXP2(m2 - mm);
#pragma unroll
    for (int i = 0; i < 16; ++i) O[i] = O[i] * f1 + cx[(wi * 17 + i) * 64 + lane] * f2;
    const float l = hi ? O[8] : O[12];
    const float rl = 1.f / l;
    const int col0 = head * DH + 4 * hi;
#pragma unroll
    for (int q = 0; q < 3; ++q) {
      if (q < 2 || hi == 0) {
        u32x2 o = {pack2(O[4 * q] * rl, O[4 * q + 1] * rl), pack2(O[4 * q + 2] * rl, O[4 * q + 3] * rl)};
        *(u32x2*)(Op + apack_off(tok, col0 + 8 * q, 20)) = o;
      }
    }
  }
}

enum { EPI_WO = 0, EPI_W1 = 1, EPI_W2 = 2, EPI_W3 = 3, EPI_W4 = 4, EPI_QKV = 5 };

template <int N> DI void wait_vm_barrier() {
  if (N == 0) asm volatile("s_waitcnt vmcnt(0) lgkmcnt(0)\n\ts_barrier" ::: "memory");
  else if (N == 9) asm volatile("s_waitcnt vmcnt(9) lgkmcnt(0)\n\ts_barrier" ::: "memory");
  else if (N == 18) asm volatile("s_waitcnt vmcnt(18) lgkmcnt(0)\n\ts_barrier" ::: "memory");
  else if (N == 27) asm volatile("s_waitcnt vmcnt(27) lgkmcnt(0)\n\ts_barrier" ::: "memory");
  else if (N == 10) asm volatile("s_waitcnt vmcnt(10) lgkmcnt(0)\n\ts_barrier" ::: "memory");
  else asm volatile("s_waitcnt vmcnt(0) lgkmcnt(0)\n\ts_barrier" ::: "memory");
}

template <int EPI> struct GCfg {
  static constexpr int MPW = (EPI == EPI_W3) ? 2 : 1;
  static constexpr int NBMAX = (EPI == EPI_W3) ? 7 : (EPI == EPI_QKV) ? 6 : 5;
  static constexpr int RING = (EPI == EPI_W3) ? 2 : (EPI == EPI_QKV) ? 3 : 4;
  static constexpr int LDSB = RING * 4 * (4 * MPW + NBMAX) * 1024 + 1024;
};

template <int EPI, int NB>
DI void gemm_body(char* lds, const u16* __restrict__ A, const u16* __restrict__ Bp, const u16* __restrict__ r1,
                  const u16* __restrict__ r2, const float* __restrict__ stin, float* __restrict__ stout, u16* __restrict__ out,
                  char* __restrict__ KVo) {
  constexpr bool LN = (EPI == EPI_W1 || EPI == EPI_W3 || EPI == EPI_QKV);
  constexpr bool NEEDST = (EPI == EPI_W1 || EPI == EPI_W2 || EPI == EPI_W3 || EPI == EPI_QKV);
  constexpr int MPW = GCfg<EPI>::MPW;
  constexpr int MBT = 4 * MPW;
  constexpr int KST = (EPI == EPI_W4) ? 54 : 20;
  constexpr int NST = (KST + 3) / 4;
  constexpr int PPK = MBT + NB;
  constexpr int RING = GCfg<EPI>::RING;
  constexpr int SLOT = 4 * PPK * 1024;

  const int tid = threadIdx.x, lane = tid & 63;
  const int w = __builtin_amdgcn_readfirstlane(tid >> 6);
  const int g = lane >> 4, r16 = lane & 15;
  const int b = blockIdx.x, xc = b & 7, bi = b >> 3;
  int mblk0, nblk0, nbcnt, nt;
  if (EPI == EPI_W3) {
    const int mt = xc * 2 + (bi >> 4); nt = bi & 15;
    mblk0 = mt * 8;
    nblk0 = nt < 12 ? 7 * nt : 84 + 6 * (nt - 12);
    nbcnt = nt < 12 ? 7 : 6;
  } else {
    const int mt = xc * 4 + (bi >> 3); nt = bi & 7;
    mblk0 = mt * 4; nblk0 = nt * 5; nbcnt = 5;
  }
  f32x4 stv[MPW][4];
  u32x2 ra[MPW][NB], rb[MPW][NB];
#pragma unroll
  for (int o = 0; o < MPW; ++o) {
    const int tok = (mblk0 + w * MPW + o) * 16 + r16;
    if (NEEDST) {
#pragma unroll
      for (int q = 0; q < 4; ++q) stv[o][q] = *(const f32x4*)(stin + (size_t)tok * 16 + q * 4);
    }
#pragma unroll
    for (int nb = 0; nb < NB; ++nb) {
      const int col = (nblk0 + nb) * 16 + 4 * g;
      if (EPI == EPI_WO) ra[o][nb] = *(const u32x2*)(r1 + (size_t)tok * D + col);
      if (EPI == EPI_W2 || EPI == EPI_W4) ra[o][nb] = *(const u32x2*)(r1 + apack_off(tok, col, 20));
      if (EPI == EPI_W4) rb[o][nb] = *(const u32x2*)(r2 + apack_off(tok, col, 20));
    }
  }
  __builtin_amdgcn_sched_barrier(0);

  const unsigned lds0 = lds_addr(lds);
  const u16* Ab = A + ((size_t)mblk0 * KST + w) * 512 + lane * 8;
  const u16* Bb = Bp + ((size_t)nblk0 * KST + w) * 512 + lane * 8;
  auto stage_part = [&](int s, int q) {
    if (KST % 4 == 0 || 4 * s + 3 < KST || 4 * s + w < KST) {
      const unsigned dst = __builtin_amdgcn_readfirstlane(lds0 + (s % RING) * SLOT + w * (PPK * 1024));
#pragma unroll
      for (int j = 0; j < PPK; ++j) {
        if (j * 4 / PPK == q) {
          if (j < MBT) glds16(Ab + ((size_t)j * KST + 4 * s) * 512, dst + j * 1024);
          else {
            const int nb = j - MBT;
            const int nn = (EPI == EPI_W3) ? (nb < nbcnt ? nb : nbcnt - 1) : (EPI == EPI_QKV && nb == 5) ? 40 + nt - nblk0 : nb;
            glds16(Bb + ((size_t)nn * KST + 4 * s) * 512, dst + j * 1024);
          }
        }
      }
    }
  };
  constexpr int PRE = RING - 1;
#pragma unroll
  for (int s = 0; s < PRE; ++s)
    if (s < NST) {
#pragma unroll
      for (int q = 0; q < 4; ++q) stage_part(s, q);
    }

  f32x4 acc[MPW][NB], cs[2];
#pragma unroll
  for (int nb = 0; nb < NB; ++nb)
#pragma unroll
    for (int o = 0; o < MPW; ++o) acc[o][nb] = (f32x4){0.f, 0.f, 0.f, 0.f};
  cs[0] = (f32x4){0.f, 0.f, 0.f, 0.f}; cs[1] = cs[0];
  const u32x4 ones4 = {0x3F803F80u, 0x3F803F80u, 0x3F803F80u, 0x3F803F80u};
  const bf16x8 ones = __builtin_bit_cast(bf16x8, ones4);
  const int csj0 = MBT + w, csj1 = MBT + ((w + 4 < NB) ? w + 4 : w);

#pragma unroll
  for (int s = 0; s < NST; ++s) {
    const int lastiss = (s + PRE - 1 < NST - 1) ? s + PRE - 1 : NST - 1;
    const int inflight = lastiss - s;
    const bool tail = (KST % 4 != 0) && (lastiss == NST - 1);
    if (tail || inflight == 0) wait_vm_barrier<0>();
    else if (inflight * PPK == 9) wait_vm_barrier<9>();
    else if (inflight * PPK == 18) wait_vm_barrier<18>();
    else if (inflight * PPK == 10) wait_vm_barrier<10>();
    else wait_vm_barrier<0>();
    const char* sb = lds + (s % RING) * SLOT + lane * 16;
    bf16x8 af[4][MPW], bfr[4][NB], cf[4][2];
#pragma unroll
    for (int kl = 0; kl < 4; ++kl) {
      if (4 * s + kl < KST) {
#pragma unroll
        for (int o = 0; o < MPW; ++o) af[kl][o] = *(const bf16x8*)(sb + (kl * PPK + w * MPW + o) * 1024);
#pragma unroll
        for (int nb = 0; nb < NB; ++nb) bfr[kl][nb] = *(const bf16x8*)(sb + (kl * PPK + MBT + nb) * 1024);
        if (LN) { cf[kl][0] = *(const bf16x8*)(sb + (kl * PPK + csj0) * 1024); cf[kl][1] = *(const bf16x8*)(sb + (kl * PPK + csj1) * 1024); }
      }
    }
    __builtin_amdgcn_sched_barrier(0);
#pragma unroll
    for (int kl = 0; kl < 4; ++kl) {
      if (4 * s + kl < KST) {
#pragma unroll
        for (int nb = 0; nb < NB; ++nb)
#pragma unroll
          for (int o = 0; o < MPW; ++o) acc[o][nb] = MFMA16(bfr[kl][nb], af[kl][o], acc[o][nb]);
        if (LN) { cs[0] = MFMA16(cf[kl][0], ones, cs[0]); cs[1] = MFMA16(cf[kl][1], ones, cs[1]); }
      }
      if (s + PRE < NST) { __builtin_amdgcn_sched_barrier(0); stage_part(s + PRE, kl); }
    }
  }
  f32x4 c[NB];
  if (LN) {
    float* csl = (float*)(lds + GCfg<EPI>::LDSB - 1024);
    if (r16 == 0) {
      *(f32x4*)(csl + w * 16 + 4 * g) = cs[0];
      if (w + 4 < NB) *(f32x4*)(csl + (w + 4) * 16 + 4 * g) = cs[1];
    }
    __syncthreads();
#pragma unroll
    for (int nb = 0; nb < NB; ++nb) c[nb] = *(const f32x4*)(csl + nb * 16 + 4 * g);
  }

#pragma unroll
  for (int o = 0; o < MPW; ++o) {
    const int tok = (mblk0 + w * MPW + o) * 16 + r16;
    float mean = 0.f, rs = 0.f;
    if (NEEDST) {
      float t1 = 0.f, t2 = 0.f;
#pragma unroll
      for (int q = 0; q < 4; ++q) { t1 += stv[o][q][0] + stv[o][q][2]; t2 += stv[o][q][1] + stv[o][q][3]; }
      mean = t1 * (1.f / D);
      const float var = fmaxf(t2 * (1.f / D) - mean * mean, 0.f);
      rs = __builtin_amdgcn_rsqf(var + LN_EPS);
      if (EPI == EPI_W1) rs = rs * __builtin_amdgcn_rsqf(var * rs * rs + LN_EPS);
    }
    float q1 = 0.f, q2 = 0.f;
#pragma unroll
    for (int nb = 0; nb < NB; ++nb) {
      const int col = (nblk0 + nb) * 16 + 4 * g;
      float z[4];
      if (EPI == EPI_WO) {
        const float xr[4] = {bflo(ra[o][nb][0]), bfhi(ra[o][nb][0]), bflo(ra[o][nb][1]), bfhi(ra[o][nb][1])};
#pragma unroll
        for (int r = 0; r < 4; ++r) { z[r] = rbf(xr[r] + rbf(acc[o][nb][r])); q1 += z[r]; q2 += z[r] * z[r]; }
        u32x2 ov = {pack2(z[0], z[1]), pack2(z[2], z[3])};
        *(u32x2*)(out + apack_off(tok, col, 20)) = ov;
      } else if (EPI == EPI_W1) {
#pragma unroll
        for (int r = 0; r < 4; ++r) z[r] = gelu_t(rbf((acc[o][nb][r] - mean * c[nb][r]) * rs));
        u32x2 ov = {pack2(z[0], z[1]), pack2(z[2], z[3])};
        const int grp = nt >> 1;
        const int sh = (grp == 0) ? 1 : (grp == 1) ? -1 : (grp == 2) ? 2 : -2;
        const int t2 = tok + sh;
        if (t2 >= 0 && t2 < S) *(u32x2*)(out + apack_off(t2, col, 20)) = ov;
        if (tok - sh < 0 || tok - sh >= S) *(u32x2*)(out + apack_off(tok, col, 20)) = (u32x2){0u, 0u};
      } else if (EPI == EPI_W2) {
        const float hr[4] = {bflo(ra[o][nb][0]), bfhi(ra[o][nb][0]), bflo(ra[o][nb][1]), bfhi(ra[o][nb][1])};
#pragma unroll
        for (int r = 0; r < 4; ++r) { z[r] = rbf(rbf((hr[r] - mean) * rs) + rbf(acc[o][nb][r])); q1 += z[r]; q2 += z[r] * z[r]; }
        u32x2 ov = {pack2(z[0], z[1]), pack2(z[2], z[3])};
        *(u32x2*)(out + apack_off(tok, col, 20)) = ov;
      } else if (EPI == EPI_W3) {
#pragma unroll
        for (int r = 0; r < 4; ++r) z[r] = gelu_t(rbf((acc[o][nb][r] - mean * c[nb][r]) * rs));
        u32x2 ov = {pack2(z[0], z[1]), pack2(z[2], z[3])};
        if (nb < nbcnt) *(u32x2*)(out + apack_off(tok, col, 54)) = ov;
      } else if (EPI == EPI_QKV) {
        if (nb < 5) {
#pragma unroll
          for (int r = 0; r < 4; ++r) z[r] = (acc[o][nb][r] - mean * c[nb][r]) * (rs * QSCALE);
          u32x2 ov = {pack2(z[0], z[1]), pack2(z[2], z[3])};
          *(u32x2*)(out + (size_t)tok * D + col) = ov;
        } else {
#pragma unroll
          for (int r = 0; r < 4; ++r) z[r] = (acc[o][nb][r] - mean * c[nb][r]) * rs;
          const int c4 = nt * 4 + g;
          if (nt < 2) {
            float nn = 0.f;
            if (c4 < 5) {
#pragma unroll
              for (int r = 0; r < 4; ++r) { const float t = rbf(z[r]); nn += t * t; }
            }
            nn += __shfl_xor(nn, 16); nn += __shfl_xor(nn, 32);
#pragma unroll
            for (int of = 1; of < 16; of <<= 1) nn = fmaxf(nn, __shfl_xor(nn, of));
            if (lane == 0) stout[nt * 128 + (tok >> 4)] = nn;
          }
          char* tb = KVo + (size_t)(tok >> 5) * KV_TILE;
          const int key = tok & 31;
          if (c4 < 4) {
            u32x2 ov = {pack2(z[0], z[1]), pack2(z[2], z[3])};
            *(u32x2*)(tb + (key + 32 * (c4 >> 1)) * 16 + (c4 & 1) * 8) = ov;
          } else if (c4 == 4) {
            u32x2 ov = {pack2(z[0], z[1]), pack2(z[2], z[3])};
            *(u32x2*)(tb + KV_K1 + key * 8) = ov;
          } else if (c4 < 10) {
            const int sk = key >> 4, hh = (key >> 2) & 1, j = ((key >> 3) & 1) * 4 + (key & 3);
            u16* vb = (u16*)(tb + KV_V) + ((sk * 2 + hh) * 21 + 4 * (c4 - 5)) * 8 + j;
#pragma unroll
            for (int r = 0; r < 4; ++r) vb[r * 8] = (u16)(pack2(z[r], 0.f) & 0xffffu);
            if (c4 == 9) vb[4 * 8] = (u16)0x3F80;
          }
        }
      } else {
        const float hr[4] = {bflo(ra[o][nb][0]), bfhi(ra[o][nb][0]), bflo(ra[o][nb][1]), bfhi(ra[o][nb][1])};
        const float xr[4] = {bflo(rb[o][nb][0]), bfhi(rb[o][nb][0]), bflo(rb[o][nb][1]), bfhi(rb[o][nb][1])};
#pragma unroll
        for (int r = 0; r < 4; ++r) z[r] = hr[r] + rbf(xr[r] + rbf(acc[o][nb][r]));
        u32x2 ov = {pack2(z[0], z[1]), pack2(z[2], z[3])};
        *(u32x2*)(out + (size_t)tok * D + col) = ov;
      }
    }
    if (EPI == EPI_WO || EPI == EPI_W2) {
      q1 += __shfl_xor(q1, 16); q2 += __shfl_xor(q2, 16);
      q1 += __shfl_xor(q1, 32); q2 += __shfl_xor(q2, 32);
      if (lane < 16) *(f32x2*)(stout + (size_t)tok * 16 + nt * 2) = (f32x2){q1, q2};
    }
  }
}

template <int EPI>
__global__ __launch_bounds__(256, 1) void gemm_kernel(const u16* __restrict__ A, const u16* __restrict__ Bp, const u16* __restrict__ r1,
                                                      const u16* __restrict__ r2, const float* __restrict__ stin,
                                                      float* __restrict__ stout, u16* __restrict__ out, char* __restrict__ KVo) {
  __shared__ __attribute__((aligned(16))) char lds[GCfg<EPI>::LDSB];
  if (EPI == EPI_QKV) {
    if (((blockIdx.x >> 3) & 7) < 3) gemm_body<EPI, 6>(lds, A, Bp, r1, r2, stin, stout, out, KVo);
    else gemm_body<EPI, 5>(lds, A, Bp, r1, r2, stin, stout, out, KVo);
  } else {
    gemm_body<EPI, GCfg<EPI>::NBMAX>(lds, A, Bp, r1, r2, stin, stout, out, KVo);
  }
}

extern "C" void kernel_launch(void* const* d_in, const int* in_sizes, int n_in, void* d_out, int out_size, void* d_ws,
                              size_t ws_size, hipStream_t stream) {
  const u16* x = (const u16*)d_in[0];
  const u16* wq = (const u16*)d_in[1];
  const u16* wk = (const u16*)d_in[2];
  const u16* wv = (const u16*)d_in[3];
  const u16* wo = (const u16*)d_in[4];
  const u16* w1 = (const u16*)d_in[6];
  const u16* w2 = (const u16*)d_in[7];
  const u16* w3 = (const u16*)d_in[8];
  const u16* w4 = (const u16*)d_in[9];
  u16* out = (u16*)d_out;
  char* ws = (char*)d_ws;
  size_t off = 0;
  auto take = [&](size_t bytes) { char* p = ws + off; off += (bytes + 4095) & ~(size_t)4095; return p; };
  u16* Qb = (u16*)take((size_t)S * D * 2);
  char* KVb = take((size_t)64 * KV_TILE + 1024);
  u16* Ob = (u16*)take((size_t)S * D * 2);
  u16* hb = (u16*)take((size_t)S * D * 2);
  u16* hhb = (u16*)take((size_t)S * D * 2);
  u16* x1b = (u16*)take((size_t)S * D * 2);
  u16* gb = (u16*)take((size_t)S * F * 2);
  u16* wop = (u16*)take((size_t)D * D * 2);
  u16* w1p = (u16*)take((size_t)D * D * 2);
  u16* w2p = (u16*)take((size_t)D * D * 2);
  u16* w3p = (u16*)take((size_t)D * F * 2);
  u16* w4p = (u16*)take((size_t)D * F * 2);
  float* sth = (float*)take((size_t)S * 16 * 4);
  float* stx = (float*)take((size_t)S * 16 * 4);
  float* kmx = (float*)take(1024);
  u16* xp = (u16*)take((size_t)S * D * 2);
  u16* wqp = (u16*)take((size_t)D * 688 * 2);
  float* st0 = (float*)take((size_t)S * 16 * 4);

  pack_kernel<<<(PW_D + 3) / 4, 256, 0, stream>>>(x, wq, wk, wv, wo, w1, w2, w3, w4, xp, st0, wqp, wop, w1p, w2p, w3p, w4p);
  gemm_kernel<EPI_QKV><<<256, 256, 0, stream>>>(xp, wqp, nullptr, nullptr, st0, kmx, Qb, KVb);
  attn_kernel<<<512, 512, 0, stream>>>(Qb, KVb, kmx, Ob);
  gemm_kernel<EPI_WO><<<256, 256, 0, stream>>>(Ob, wop, x, nullptr, nullptr, sth, hb, nullptr);
  gemm_kernel<EPI_W1><<<256, 256, 0, stream>>>(hb, w1p, nullptr, nullptr, sth, nullptr, hhb, nullptr);
  gemm_kernel<EPI_W2><<<256, 256, 0, stream>>>(hhb, w2p, hb, nullptr, sth, stx, x1b, nullptr);
  gemm_kernel<EPI_W3><<<256, 256, 0, stream>>>(x1b, w3p, nullptr, nullptr, stx, nullptr, gb, nullptr);
  gemm_kernel<EPI_W4><<<256, 256, 0, stream>>>(gb, w4p, hb, x1b, nullptr, nullptr, out, nullptr);
}
```

```cpp
#include <hip/hip_runtime.h>
#include <hip/hip_bf16.h>
#include <stdint.h>
#include <type_traits>

typedef __attribute__((ext_vector_type(8))) __bf16 bf16x8;
typedef __attribute__((ext_vector_type(2))) __bf16 bf16x2;
typedef __attribute__((ext_vector_type(4))) float f32x4;
typedef __attribute__((ext_vector_type(2))) float f32x2;
typedef __attribute__((ext_vector_type(16))) float f32x16;
typedef __attribute__((ext_vector_type(4))) unsigned u32x4;
typedef __attribute__((ext_vector_type(2))) unsigned u32x2;
typedef short v4i16 __attribute__((ext_vector_type(4)));
typedef unsigned short u16;
#define LAS __attribute__((address_space(3)))
#define DI __device__ __forceinline__

constexpr int S = 2048, D = 640, H = 32, DH = 20, F = 1728;
constexpr float LN_EPS = 1e-5f;
constexpr float QSCALE = 0.22360679774997896f * 1.4426950408889634f;
constexpr int KV_TILE = 2624;
constexpr int KV_K1 = 1024, KV_V = 1280;

DI float bf2f(unsigned u) { return __uint_as_float(u << 16); }
DI float bflo(unsigned u) { return __uint_as_float(u << 16); }
DI float bfhi(unsigned u) { return __uint_as_float(u & 0xffff0000u); }
DI unsigned pack2(float a, float b) { f32x2 f = {a, b}; bf16x2 c = __builtin_convertvector(f, bf16x2); return __builtin_bit_cast(unsigned, c); }
DI float rbf(float a) { return bflo(pack2(a, 0.f)); }
DI float gelu_t(float x) {
  const float c0 = -2.f * 0.7978845608028654f * 1.4426950408889634f;
  float u = x * __builtin_fmaf(x * x, 0.044715f, 1.f);
  float e = __builtin_amdgcn_exp2f(u * c0);
  return x * __builtin_amdgcn_rcpf(1.f + e);
}
#define MFMA16(a, b, c) __builtin_amdgcn_mfma_f32_16x16x32_bf16((a), (b), (c), 0, 0, 0)
#define MFMA32(a, b, c) __builtin_amdgcn_mfma_f32_32x32x16_bf16((a), (b), (c), 0, 0, 0)

DI void glds16(const void* g, unsigned lds_base) {
  unsigned sv;
  asm volatile("s_mov_b32 %0, m0\n\ts_mov_b32 m0, %2\n\ts_nop 0\n\tglobal_load_lds_dwordx4 %1, off\n\ts_mov_b32 m0, %0"
               : "=&s"(sv) : "v"(g), "s"(lds_base) : "memory");
}
DI unsigned lds_addr(const void* p) { return (unsigned)(size_t)(const LAS char*)p; }

DI int apack_off(int row, int col, int KS) {
  return (((row >> 4) * KS + (col >> 5)) * 64 + (row & 15) + 16 * ((col >> 3) & 3)) * 8 + (col & 7);
}

constexpr int PW_X = 512;
constexpr int PW_A = PW_X + 400;
constexpr int PW_B = PW_A + 280;
constexpr int PW_C = PW_B + 270;
constexpr int PW_D = PW_C + 20;

__global__ __launch_bounds__(256) void pack_kernel(const u16* __restrict__ x, const u16* __restrict__ wq, const u16* __restrict__ wk,
                                                   const u16* __restrict__ wv, const u16* __restrict__ wo, const u16* __restrict__ w1,
                                                   const u16* __restrict__ w2, const u16* __restrict__ w3, const u16* __restrict__ w4,
                                                   u16* __restrict__ xp, float* __restrict__ stx, u16* __restrict__ wqp,
                                                   u16* __restrict__ wop, u16* __restrict__ w1p, u16* __restrict__ w2p,
                                                   u16* __restrict__ w3p, u16* __restrict__ w4p) {
  __shared__ __attribute__((aligned(16))) char lds[4 * 8 * 1152];
  const int lane = threadIdx.x & 63;
  const int wv_ = __builtin_amdgcn_readfirstlane(blockIdx.x * 4 + (threadIdx.x >> 6));
  if (wv_ < PW_X) {
    const int mb = wv_ >> 2, kq = wv_ & 3, r16 = lane & 15, g = lane >> 4;
    u32x4 v[5];
#pragma unroll
    for (int i = 0; i < 5; ++i) v[i] = *(const u32x4*)(x + (size_t)(mb * 16 + r16) * D + (kq * 5 + i) * 32 + g * 8);
    float s1 = 0.f, s2 = 0.f;
#pragma unroll
    for (int i = 0; i < 5; ++i) {
      *(u32x4*)(xp + ((size_t)(mb * 20 + kq * 5 + i) * 64 + lane) * 8) = v[i];
#pragma unroll
      for (int j = 0; j < 4; ++j) { const float e0 = bflo(v[i][j]), e1 = bfhi(v[i][j]); s1 += e0 + e1; s2 += e0 * e0 + e1 * e1; }
    }
    s1 += __shfl_xor(s1, 16); s2 += __shfl_xor(s2, 16);
    s1 += __shfl_xor(s1, 32); s2 += __shfl_xor(s2, 32);
    *(f32x2*)(stx + (size_t)(mb * 16 + r16) * 32 + (kq + 4 * g) * 2) = (g == 0) ? (f32x2){s1, s2} : (f32x2){0.f, 0.f};
    return;
  }
  if (wv_ >= PW_D) return;
  const int blk = lane >> 3, no2 = lane & 1, kg = (lane >> 1) & 3;
  const u16* W; u16* P; int N, KS, ks, nb0, nblk = 8;
  bool kvt = false;
  if (wv_ < PW_A) {
    const int u = wv_ - PW_X, which = u / 100, uu = u % 100;
    W = which == 0 ? wq : which == 1 ? wo : which == 2 ? w1 : w2;
    P = which == 0 ? wqp : which == 1 ? wop : which == 2 ? w1p : w2p;
    N = D; KS = 20; ks = uu / 5; nb0 = (uu % 5) * 8;
  } else if (wv_ < PW_B) {
    const int u = wv_ - PW_A; W = w3; P = w3p; N = F; KS = 20; ks = u / 14; nb0 = (u % 14) * 8; if (nb0 == 104) nblk = 4;
  } else if (wv_ < PW_C) {
    const int u = wv_ - PW_B; W = w4; P = w4p; N = D; KS = 54; ks = u / 5; nb0 = (u % 5) * 8;
  } else {
    W = wk; P = wqp; N = DH; KS = 20; ks = wv_ - PW_C; nb0 = 40; nblk = 3; kvt = true;
  }
  u32x4 r[8];
  if (!kvt) {
    const int bb = blk < nblk ? blk : 0;
    const u16* src = W + (size_t)(ks * 32 + kg * 8) * N + (nb0 + bb) * 16 + no2 * 8;
#pragma unroll
    for (int i = 0; i < 8; ++i) r[i] = *(const u32x4*)(src + (size_t)i * N);
  } else {
    const u16* sp[2]; unsigned msk[2];
#pragma unroll
    for (int h2 = 0; h2 < 2; ++h2) {
      const int c4 = blk * 4 + no2 * 2 + h2;
      sp[h2] = (c4 < 5 ? wk + 4 * c4 : wv + 4 * (c4 < 10 ? c4 - 5 : 0)) + (size_t)(ks * 32 + kg * 8) * DH;
      msk[h2] = c4 < 10 ? 0xffffffffu : 0u;
    }
#pragma unroll
    for (int i = 0; i < 8; ++i) {
#pragma unroll
      for (int h2 = 0; h2 < 2; ++h2) {
        const u32x2 e = *(const u32x2*)(sp[h2] + i * DH);
        r[i][2 * h2] = e[0] & msk[h2]; r[i][2 * h2 + 1] = e[1] & msk[h2];
      }
    }
  }
  char* wl = lds + (threadIdx.x >> 6) * (8 * 1152);
  {
    char* d = wl + blk * 1152 + (no2 + 2 * kg) * 144;
#pragma unroll
    for (int i = 0; i < 8; ++i) {
      u32x4 o;
#pragma unroll
      for (int p = 0; p < 4; ++p)
        o[p] = __builtin_amdgcn_perm(r[2 * p + 1][i >> 1], r[2 * p][i >> 1], (i & 1) ? 0x07060302u : 0x05040100u);
      *(u32x4*)(d + i * 16) = o;
    }
  }
  asm volatile("s_waitcnt lgkmcnt(0)" ::: "memory");
#pragma unroll
  for (int bq = 0; bq < 8; ++bq) {
    if (bq < nblk) {
      const u32x4 o = *(const u32x4*)(wl + bq * 1152 + (lane >> 3) * 144 + (lane & 7) * 16);
      *(u32x4*)(P + ((size_t)((nb0 + bq) * KS + ks) * 64 + lane) * 8) = o;
    }
  }
}

constexpr int ACH = 4 * KV_TILE;
constexpr float ATHR = 16.f;
#define SB() __builtin_amdgcn_sched_barrier(0)
#define EXP2(x) __builtin_amdgcn_exp2f(x)

template <bool ONLINE>
DI void attn_pass(const char* __restrict__ KV, char* lds, int lane, int wi, int half, const u32x4 q0u, u32x4 q1u, f32x16& O,
                  float& mcur) {
  const int hi = lane >> 5, l32 = lane & 31;
  const unsigned lds0 = lds_addr(lds);
  auto stage = [&](int c, int buf) {
    const char* src = KV + (size_t)(half * 32 + c * 4) * KV_TILE + lane * 16;
    const unsigned dst = __builtin_amdgcn_readfirstlane(lds0 + (buf * 2 + half) * ACH + wi * 1024);
    glds16(src + wi * 1024, dst);
    glds16(src + (wi + 4) * 1024, dst + 4096);
    if (wi < 2) glds16(src + (wi + 8) * 1024, dst + 8192);
    if (wi == 2 && lane < 16) glds16(src + 10 * 1024, dst + 8192);
  };
  stage(0, 0); stage(1, 1);
  f32x16 Z;
#pragma unroll
  for (int i = 0; i < 16; ++i) { O[i] = 0.f; Z[i] = 0.f; }
  const int koff = lane * 16, k1off = KV_K1 + l32 * 8;
  const int voff = KV_V + (hi * 21 + (l32 < 20 ? l32 : 20)) * 16;
  asm volatile("s_waitcnt vmcnt(0)" ::: "memory");
  __syncthreads();
  const char* hb = lds + half * ACH;
  f32x16 Sx;
  bf16x8 k0n, v0, v1; u32x2 k1n;
  {
    const bf16x8 k0 = *(const bf16x8*)(hb + koff);
    const u32x2 k1d = *(const u32x2*)(hb + k1off);
    const u32x4 k1u = {k1d[0], k1d[1], 0x00003F80u, 0u};
    Sx = MFMA32(k0, __builtin_bit_cast(bf16x8, q0u), Z);
    Sx = MFMA32(__builtin_bit_cast(bf16x8, k1u), __builtin_bit_cast(bf16x8, q1u), Sx);
    k0n = *(const bf16x8*)(hb + KV_TILE + koff); k1n = *(const u32x2*)(hb + KV_TILE + k1off);
    v0 = *(const bf16x8*)(hb + voff); v1 = *(const bf16x8*)(hb + voff + 672);
    if (ONLINE) {
      float mx = fmaxf(fmaxf(Sx[0], Sx[1]), Sx[2]);
#pragma unroll
      for (int i = 3; i < 15; i += 2) mx = fmaxf(fmaxf(mx, Sx[i]), Sx[i + 1]);
      mx = fmaxf(mx, Sx[15]);
      const float mo = fmaxf(mx, __shfl_xor(mx, 32));
      mcur = rbf(mo);
#pragma unroll
      for (int i = 0; i < 16; ++i) Sx[i] -= mcur;
      q1u[2] = hi ? 0u : (pack2(-mcur, 0.f) & 0xffffu);
    }
  }
  int bcur = 0;
  for (int c = 0; c < 8; ++c) {
    const int bn1 = (bcur == 2) ? 0 : bcur + 1, bn2 = (bn1 == 2) ? 0 : bn1 + 1;
    if (c + 2 < 8) stage(c + 2, bn2);
    const char* cb = hb + bcur * (2 * ACH);
    const char* nb = hb + bn1 * (2 * ACH);
#pragma unroll
    for (int t = 0; t < 4; ++t) {
      const char* tv = (t < 3) ? cb + (t + 1) * KV_TILE : nb;
      const char* tk = (t < 2) ? cb + (t + 2) * KV_TILE : nb + (t - 2) * KV_TILE;
      u32x4 p0, p1;
      f32x16 Sn;
      SB();
      const u32x4 k1u = {k1n[0], k1n[1], 0x00003F80u, 0u};
      Sn = MFMA32(k0n, __builtin_bit_cast(bf16x8, q0u), Z);
      SB();
      p0[0] = pack2(EXP2(Sx[0]), EXP2(Sx[1])); p0[1] = pack2(EXP2(Sx[2]), EXP2(Sx[3]));
      SB();
      Sn = MFMA32(__builtin_bit_cast(bf16x8, k1u), __builtin_bit_cast(bf16x8, q1u), Sn);
      SB();
      p0[2] = pack2(EXP2(Sx[4]), EXP2(Sx[5])); p0[3] = pack2(EXP2(Sx[6]), EXP2(Sx[7]));
      SB();
      O = MFMA32(v0, __builtin_bit_cast(bf16x8, p0), O);
      k0n = *(const bf16x8*)(tk + koff); k1n = *(const u32x2*)(tk + k1off);
      v0 = *(const bf16x8*)(tv + voff);
      SB();
      p1[0] = pack2(EXP2(Sx[8]), EXP2(Sx[9])); p1[1] = pack2(EXP2(Sx[10]), EXP2(Sx[11]));
      p1[2] = pack2(EXP2(Sx[12]), EXP2(Sx[13])); p1[3] = pack2(EXP2(Sx[14]), EXP2(Sx[15]));
      SB();
      O = MFMA32(v1, __builtin_bit_cast(bf16x8, p1), O);
      v1 = *(const bf16x8*)(tv + voff + 672);
      SB();
      if (ONLINE) {
        float mx = fmaxf(fmaxf(Sn[0], Sn[1]), Sn[2]);
#pragma unroll
        for (int i = 3; i < 15; i += 2) mx = fmaxf(fmaxf(mx, Sn[i]), Sn[i + 1]);
        mx = fmaxf(mx, Sn[15]);
        if (!(c == 7 && t == 3) && __any(mx > ATHR)) {
          const float mo = fmaxf(mx, __shfl_xor(mx, 32));
          const float mnew = rbf(mcur + fmaxf(mo, 0.f));
          const float delta = mnew - mcur;
          const float sc = EXP2(-delta);
#pragma unroll
          for (int i = 0; i < 16; ++i) { Sn[i] -= delta; O[i] *= sc; }
          mcur = mnew;
          q1u[2] = hi ? 0u : (pack2(-mnew, 0.f) & 0xffffu);
        }
      }
      Sx = Sn;
    }
    asm volatile("s_waitcnt vmcnt(0)" ::: "memory");
    __syncthreads();
    bcur = bn1;
  }
}

__global__ __launch_bounds__(512, 4) void attn_kernel(const u16* __restrict__ Q, const char* __restrict__ KV,
                                                      const float* __restrict__ kmax, u16* __restrict__ Op) {
  __shared__ __attribute__((aligned(16))) char lds[6 * ACH];
  const int tid = threadIdx.x, lane = tid & 63;
  const int w = __builtin_amdgcn_readfirstlane(tid >> 6);
  const int hi = lane >> 5, l32 = lane & 31;
  const int b = blockIdx.x, xc = b & 7, bi = b >> 3;
  const int tb = xc * 8 + (bi >> 3), hg = bi & 7;
  const int head = hg * 4 + (w & 3), half = w >> 2, wi = w & 3;
  const int tok = tb * 32 + l32;
  const u16* qp = Q + (size_t)tok * D + head * DH;
  u32x4 q0u, q1u;
  {
    const u32x2 a = *(const u32x2*)(qp + 8 * hi), c = *(const u32x2*)(qp + 8 * hi + 4);
    q0u = (u32x4){a[0], a[1], c[0], c[1]};
    u32x2 e = *(const u32x2*)(qp + 16);
    q1u = (u32x4){hi ? 0u : e[0], hi ? 0u : e[1], 0u, 0u};
  }
  float mcur;
  {
    const f32x2 ka = *(const f32x2*)(kmax + lane * 2), kb = *(const f32x2*)(kmax + 128 + lane * 2);
    float ma = fmaxf(ka[0], ka[1]), mb = fmaxf(kb[0], kb[1]);
#pragma unroll
    for (int o = 1; o < 64; o <<= 1) { ma = fmaxf(ma, __shfl_xor(ma, o)); mb = fmaxf(mb, __shfl_xor(mb, o)); }
    float qq = 0.f;
#pragma unroll
    for (int i = 0; i < 4; ++i) { qq += bflo(q0u[i]) * bflo(q0u[i]) + bfhi(q0u[i]) * bfhi(q0u[i]); }
#pragma unroll
    for (int i = 0; i < 2; ++i) { qq += bflo(q1u[i]) * bflo(q1u[i]) + bfhi(q1u[i]) * bfhi(q1u[i]); }
    qq += __shfl_xor(qq, 32);
    mcur = rbf(__builtin_sqrtf(qq * (ma + mb)) * 1.02f + 1e-30f);
    q1u[2] = hi ? 0u : (pack2(-mcur, 0.f) & 0xffffu);
  }
  f32x16 O;
  attn_pass<false>(KV, lds, lane, wi, half, q0u, q1u, O, mcur);
  {
    const float l = hi ? O[8] : O[12];
    const int bad = !(l >= 8.6736174e-19f && l < 1e30f);
    if (__syncthreads_or(bad)) {
      q1u[2] = 0u;
      attn_pass<true>(KV, lds, lane, wi, half, q0u, q1u, O, mcur);
    }
  }
  float* cx = (float*)lds;
  if (half == 1) {
#pragma unroll
    for (int i = 0; i < 16; ++i) cx[(wi * 17 + i) * 64 + lane] = O[i];
    cx[(wi * 17 + 16) * 64 + lane] = mcur;
  }
  __syncthreads();
  if (half == 0) {
    const float m2 = cx[(wi * 17 + 16) * 64 + lane];
    const float mm = fmaxf(mcur, m2);
    const float f1 = EXP2(mcur - mm), f2 = EXP2(m2 - mm);
#pragma unroll
    for (int i = 0; i < 16; ++i) O[i] = O[i] * f1 + cx[(wi * 17 + i) * 64 + lane] * f2;
    const float l = hi ? O[8] : O[12];
    const float rl = 1.f / l;
    const int col0 = head * DH + 4 * hi;
#pragma unroll
    for (int q = 0; q < 3; ++q) {
      if (q < 2 || hi == 0) {
        u32x2 o = {pack2(O[4 * q] * rl, O[4 * q + 1] * rl), pack2(O[4 * q + 2] * rl, O[4 * q + 3] * rl)};
        *(u32x2*)(Op + apack_off(tok, col0 + 8 * q, 20)) = o;
      }
    }
  }
}

enum { EPI_WO = 0, EPI_W1 = 1, EPI_W2 = 2, EPI_W3 = 3, EPI_W4 = 4, EPI_QKV = 5 };

template <int N> DI void wait_vm_barrier() {
  asm volatile("s_waitcnt vmcnt(%0)\n\ts_barrier" :: "i"(N) : "memory");
}

template <int I, int CNT> DI void drain_tail() {
  wait_vm_barrier<I * CNT>();
  if constexpr (I > 0) drain_tail<I - 1, CNT>();
}

template <int EPI> struct GCfg {
  static constexpr int MPW = (EPI == EPI_W3) ? 2 : 1;
  static constexpr int NBMAX = (EPI == EPI_W3) ? 7 : (EPI == EPI_QKV) ? 6 : 5;
  static constexpr int PRE = (EPI == EPI_W3) ? 2 : 3;
  static constexpr int LDSB = (PRE + 1) * 2 * (4 * MPW + NBMAX) * 1024 + 1024;
};

template <int EPI, int NB, int NS, int NC>
DI void gemm_compute(char* lds, const u16* __restrict__ r1, const u16* __restrict__ r2, const float* __restrict__ stin,
                     float* __restrict__ stout, u16* __restrict__ out, char* __restrict__ KVo, int w, int mblk0, int nblk0,
                     int nbcnt, int nt) {
  constexpr bool LN = (EPI == EPI_W1 || EPI == EPI_W3 || EPI == EPI_QKV);
  constexpr bool NEEDST = (EPI == EPI_W1 || EPI == EPI_W2 || EPI == EPI_W3 || EPI == EPI_QKV);
  constexpr int MPW = GCfg<EPI>::MPW, MBT = 4 * MPW;
  constexpr int KST = (EPI == EPI_W4) ? 54 : 20, NST = KST / 2;
  constexpr int PPK = MBT + NB, RING = GCfg<EPI>::PRE + 1, SLOT = 2 * PPK * 1024;
  const int lane = threadIdx.x & 63, g = lane >> 4, r16 = lane & 15;
  const int wm = w & 3;

  f32x4 stv[MPW][2];
  u32x2 ra[MPW][NC], rb[MPW][NC];
#pragma unroll
  for (int o = 0; o < MPW; ++o) {
    const int tok = (mblk0 + wm * MPW + o) * 16 + r16;
    if (NEEDST) {
#pragma unroll
      for (int q = 0; q < 2; ++q) stv[o][q] = *(const f32x4*)(stin + (size_t)(tok - r16) * 32 + q * 256 + lane * 4);
    }
#pragma unroll
    for (int nb = 0; nb < NC; ++nb) {
      const int col = (nblk0 + NS + nb) * 16 + 4 * g;
      if (EPI == EPI_WO) ra[o][nb] = *(const u32x2*)(r1 + (size_t)tok * D + col);
      if (EPI == EPI_W2 || EPI == EPI_W4) ra[o][nb] = *(const u32x2*)(r1 + apack_off(tok, col, 20));
      if (EPI == EPI_W4) rb[o][nb] = *(const u32x2*)(r2 + apack_off(tok, col, 20));
    }
  }
  __builtin_amdgcn_sched_barrier(0);

  f32x4 acc[MPW][NC], cs = {0.f, 0.f, 0.f, 0.f};
#pragma unroll
  for (int nb = 0; nb < NC; ++nb)
#pragma unroll
    for (int o = 0; o < MPW; ++o) acc[o][nb] = (f32x4){0.f, 0.f, 0.f, 0.f};
  const u32x4 ones4 = {0x3F803F80u, 0x3F803F80u, 0x3F803F80u, 0x3F803F80u};
  const bf16x8 ones = __builtin_bit_cast(bf16x8, ones4);
  const int csj = MBT + (w < NB ? w : 0);

  int slot = 0;
  for (int s = 0; s < NST; ++s) {
    asm volatile("s_waitcnt lgkmcnt(0)\n\ts_barrier" ::: "memory");
    const char* sb = lds + slot * SLOT + lane * 16;
    slot = (slot == RING - 1) ? 0 : slot + 1;
    bf16x8 af[2][MPW], bfr[2][NC], cf[2];
#pragma unroll
    for (int kl = 0; kl < 2; ++kl) {
#pragma unroll
      for (int o = 0; o < MPW; ++o) af[kl][o] = *(const bf16x8*)(sb + (kl * PPK + wm * MPW + o) * 1024);
#pragma unroll
      for (int nb = 0; nb < NC; ++nb) bfr[kl][nb] = *(const bf16x8*)(sb + (kl * PPK + MBT + NS + nb) * 1024);
      if (LN) cf[kl] = *(const bf16x8*)(sb + (kl * PPK + csj) * 1024);
    }
    __builtin_amdgcn_sched_barrier(0);
#pragma unroll
    for (int kl = 0; kl < 2; ++kl) {
#pragma unroll
      for (int nb = 0; nb < NC; ++nb)
#pragma unroll
        for (int o = 0; o < MPW; ++o) acc[o][nb] = MFMA16(bfr[kl][nb], af[kl][o], acc[o][nb]);
      if (LN) cs = MFMA16(cf[kl], ones, cs);
    }
  }
  f32x4 c[NC];
  if (LN) {
    float* csl = (float*)(lds + GCfg<EPI>::LDSB - 1024);
    if (r16 == 0 && w < NB) *(f32x4*)(csl + w * 16 + 4 * g) = cs;
    __syncthreads();
#pragma unroll
    for (int nb = 0; nb < NC; ++nb) c[nb] = *(const f32x4*)(csl + (NS + nb) * 16 + 4 * g);
  }

#pragma unroll
  for (int o = 0; o < MPW; ++o) {
    const int tok = (mblk0 + wm * MPW + o) * 16 + r16;
    float mean = 0.f, rs = 0.f;
    if (NEEDST) {
      float t1 = 0.f, t2 = 0.f;
#pragma unroll
      for (int q = 0; q < 2; ++q) {
        float u1 = stv[o][q][0] + stv[o][q][2], u2 = stv[o][q][1] + stv[o][q][3];
#pragma unroll
        for (int of = 1; of < 8; of <<= 1) { u1 += __shfl_xor(u1, of); u2 += __shfl_xor(u2, of); }
        const float v1 = __shfl(u1, (r16 & 7) * 8), v2 = __shfl(u2, (r16 & 7) * 8);
        if ((r16 >> 3) == q) { t1 = v1; t2 = v2; }
      }
      mean = t1 * (1.f / D);
      const float var = fmaxf(t2 * (1.f / D) - mean * mean, 0.f);
      rs = __builtin_amdgcn_rsqf(var + LN_EPS);
      if (EPI == EPI_W1) rs = rs * __builtin_amdgcn_rsqf(var * rs * rs + LN_EPS);
    }
    float q1 = 0.f, q2 = 0.f;
#pragma unroll
    for (int nb = 0; nb < NC; ++nb) {
      const int col = (nblk0 + NS + nb) * 16 + 4 * g;
      float z[4];
      if (EPI == EPI_WO) {
        const float xr[4] = {bflo(ra[o][nb][0]), bfhi(ra[o][nb][0]), bflo(ra[o][nb][1]), bfhi(ra[o][nb][1])};
#pragma unroll
        for (int r = 0; r < 4; ++r) { z[r] = rbf(xr[r] + rbf(acc[o][nb][r])); q1 += z[r]; q2 += z[r] * z[r]; }
        u32x2 ov = {pack2(z[0], z[1]), pack2(z[2], z[3])};
        *(u32x2*)(out + apack_off(tok, col, 20)) = ov;
      } else if (EPI == EPI_W1) {
#pragma unroll
        for (int r = 0; r < 4; ++r) z[r] = gelu_t(rbf((acc[o][nb][r] - mean * c[nb][r]) * rs));
        u32x2 ov = {pack2(z[0], z[1]), pack2(z[2], z[3])};
        const int grp = nt >> 1;
        const int sh = (grp == 0) ? 1 : (grp == 1) ? -1 : (grp == 2) ? 2 : -2;
        const int t2 = tok + sh;
        if (t2 >= 0 && t2 < S) *(u32x2*)(out + apack_off(t2, col, 20)) = ov;
        if (tok - sh < 0 || tok - sh >= S) *(u32x2*)(out + apack_off(tok, col, 20)) = (u32x2){0u, 0u};
      } else if (EPI == EPI_W2) {
        const float hr[4] = {bflo(ra[o][nb][0]), bfhi(ra[o][nb][0]), bflo(ra[o][nb][1]), bfhi(ra[o][nb][1])};
#pragma unroll
        for (int r = 0; r < 4; ++r) { z[r] = rbf(rbf((hr[r] - mean) * rs) + rbf(acc[o][nb][r])); q1 += z[r]; q2 += z[r] * z[r]; }
        u32x2 ov = {pack2(z[0], z[1]), pack2(z[2], z[3])};
        *(u32x2*)(out + apack_off(tok, col, 20)) = ov;
      } else if (EPI == EPI_W3) {
#pragma unroll
        for (int r = 0; r < 4; ++r) z[r] = gelu_t(rbf((acc[o][nb][r] - mean * c[nb][r]) * rs));
        u32x2 ov = {pack2(z[0], z[1]), pack2(z[2], z[3])};
        if (NS + nb < nbcnt) *(u32x2*)(out + apack_off(tok, col, 54)) = ov;
      } else if (EPI == EPI_QKV) {
        if (NS + nb < 5) {
#pragma unroll
          for (int r = 0; r < 4; ++r) z[r] = (acc[o][nb][r] - mean * c[nb][r]) * (rs * QSCALE);
          u32x2 ov = {pack2(z[0], z[1]), pack2(z[2], z[3])};
          *(u32x2*)(out + (size_t)tok * D + col) = ov;
        } else {
#pragma unroll
          for (int r = 0; r < 4; ++r) z[r] = (acc[o][nb][r] - mean * c[nb][r]) * rs;
          const int c4 = nt * 4 + g;
          if (nt < 2) {
            float nn = 0.f;
            if (c4 < 5) {
#pragma unroll
              for (int r = 0; r < 4; ++r) { const float t = rbf(z[r]); nn += t * t; }
            }
            nn += __shfl_xor(nn, 16); nn += __shfl_xor(nn, 32);
#pragma unroll
            for (int of = 1; of < 16; of <<= 1) nn = fmaxf(nn, __shfl_xor(nn, of));
            if (lane == 0) stout[nt * 128 + (tok >> 4)] = nn;
          }
          char* tb = KVo + (size_t)(tok >> 5) * KV_TILE;
          const int key = tok & 31;
          if (c4 < 4) {
            u32x2 ov = {pack2(z[0], z[1]), pack2(z[2], z[3])};
            *(u32x2*)(tb + (key + 32 * (c4 >> 1)) * 16 + (c4 & 1) * 8) = ov;
          } else if (c4 == 4) {
            u32x2 ov = {pack2(z[0], z[1]), pack2(z[2], z[3])};
            *(u32x2*)(tb + KV_K1 + key * 8) = ov;
          } else if (c4 < 10) {
            const int sk = key >> 4, hh = (key >> 2) & 1, j = ((key >> 3) & 1) * 4 + (key & 3);
            u16* vb = (u16*)(tb + KV_V) + ((sk * 2 + hh) * 21 + 4 * (c4 - 5)) * 8 + j;
#pragma unroll
            for (int r = 0; r < 4; ++r) vb[r * 8] = (u16)(pack2(z[r], 0.f) & 0xffffu);
            if (c4 == 9) vb[4 * 8] = (u16)0x3F80;
          }
        }
      } else {
        const float hr[4] = {bflo(ra[o][nb][0]), bfhi(ra[o][nb][0]), bflo(ra[o][nb][1]), bfhi(ra[o][nb][1])};
        const float xr[4] = {bflo(rb[o][nb][0]), bfhi(rb[o][nb][0]), bflo(rb[o][nb][1]), bfhi(rb[o][nb][1])};
#pragma unroll
        for (int r = 0; r < 4; ++r) z[r] = hr[r] + rbf(xr[r] + rbf(acc[o][nb][r]));
        u32x2 ov = {pack2(z[0], z[1]), pack2(z[2], z[3])};
        *(u32x2*)(out + (size_t)tok * D + col) = ov;
      }
    }
    if (EPI == EPI_WO || EPI == EPI_W2) {
      q1 += __shfl_xor(q1, 16); q2 += __shfl_xor(q2, 16);
      q1 += __shfl_xor(q1, 32); q2 += __shfl_xor(q2, 32);
      if (lane < 16) *(f32x2*)(stout + (size_t)tok * 32 + (nt * 2 + (NS > 0 ? 1 : 0)) * 2) = (f32x2){q1, q2};
    }
  }
}

template <int EPI, int NB>
DI void gemm_body(char* lds, const u16* __restrict__ A, const u16* __restrict__ Bp, const u16* __restrict__ r1,
                  const u16* __restrict__ r2, const float* __restrict__ stin, float* __restrict__ stout, u16* __restrict__ out,
                  char* __restrict__ KVo) {
  constexpr int MPW = GCfg<EPI>::MPW, MBT = 4 * MPW;
  constexpr int KST = (EPI == EPI_W4) ? 54 : 20, NST = KST / 2;
  constexpr int PPK = MBT + NB;
  constexpr int PRE = GCfg<EPI>::PRE, RING = PRE + 1;
  constexpr int SLOT = 2 * PPK * 1024;
  const int lane = threadIdx.x & 63;
  const int w = __builtin_amdgcn_readfirstlane(threadIdx.x >> 6);
  const int b = blockIdx.x, xc = b & 7, bi = b >> 3;
  int mblk0, nblk0, nbcnt, nt;
  if (EPI == EPI_W3) {
    const int mt = xc * 2 + (bi >> 4); nt = bi & 15;
    mblk0 = mt * 8;
    nblk0 = nt < 12 ? 7 * nt : 84 + 6 * (nt - 12);
    nbcnt = nt < 12 ? 7 : 6;
  } else {
    const int mt = xc * 4 + (bi >> 3); nt = bi & 7;
    mblk0 = mt * 4; nblk0 = nt * 5; nbcnt = 5;
  }
  if (w >= 8) {
    const int kl = w & 1;
    const unsigned lds0 = lds_addr(lds) + kl * PPK * 1024;
    const u16* Ab = A + ((size_t)mblk0 * KST + kl) * 512 + lane * 8;
    const u16* Bb = Bp + ((size_t)nblk0 * KST + kl) * 512 + lane * 8;
    auto run = [&](auto J0c, auto CNTc) {
      constexpr int J0 = decltype(J0c)::value, CNT = decltype(CNTc)::value;
      auto issue = [&](int s, int slot) {
#pragma unroll
        for (int j = J0; j < J0 + CNT; ++j) {
          const unsigned dst = lds0 + slot * SLOT + j * 1024;
          if (j < MBT) glds16(Ab + ((size_t)j * KST + 2 * s) * 512, dst);
          else {
            const int nb = j - MBT;
            const int nn = (EPI == EPI_W3) ? (nb < nbcnt ? nb : nbcnt - 1) : (EPI == EPI_QKV && nb == 5) ? 40 + nt - nblk0 : nb;
            glds16(Bb + ((size_t)nn * KST + 2 * s) * 512, dst);
          }
        }
      };
#pragma unroll
      for (int s = 0; s < PRE; ++s) issue(s, s);
      int slot = PRE;
      for (int s = 0; s < NST - PRE; ++s) {
        wait_vm_barrier<(PRE - 1) * CNT>();
        issue(s + PRE, slot);
        slot = (slot == RING - 1) ? 0 : slot + 1;
      }
      drain_tail<PRE - 1, CNT>();
    };
    constexpr int H0 = (PPK + 1) / 2;
    if (w < 10) run(std::integral_constant<int, 0>{}, std::integral_constant<int, H0>{});
    else run(std::integral_constant<int, H0>{}, std::integral_constant<int, PPK - H0>{});
    return;
  }
  constexpr int N0 = (NB + 1) / 2;
  if (w < 4) gemm_compute<EPI, NB, 0, N0>(lds, r1, r2, stin, stout, out, KVo, w, mblk0, nblk0, nbcnt, nt);
  else gemm_compute<EPI, NB, N0, NB - N0>(lds, r1, r2, stin, stout, out, KVo, w, mblk0, nblk0, nbcnt, nt);
}

template <int EPI>
__global__ __launch_bounds__(768, 1) void gemm_kernel(const u16* __restrict__ A, const u16* __restrict__ Bp, const u16* __restrict__ r1,
                                                      const u16* __restrict__ r2, const float* __restrict__ stin,
                                                      float* __restrict__ stout, u16* __restrict__ out, char* __restrict__ KVo) {
  __shared__ __attribute__((aligned(16))) char lds[GCfg<EPI>::LDSB];
  if (EPI == EPI_QKV) {
    if (((blockIdx.x >> 3) & 7) < 3) gemm_body<EPI, 6>(lds, A, Bp, r1, r2, stin, stout, out, KVo);
    else gemm_body<EPI, 5>(lds, A, Bp, r1, r2, stin, stout, out, KVo);
  } else {
    gemm_body<EPI, GCfg<EPI>::NBMAX>(lds, A, Bp, r1, r2, stin, stout, out, KVo);
  }
}

extern "C" void kernel_launch(void* const* d_in, const int* in_sizes, int n_in, void* d_out, int out_size, void* d_ws,
                              size_t ws_size, hipStream_t stream) {
  const u16* x = (const u16*)d_in[0];
  const u16* wq = (const u16*)d_in[1];
  const u16* wk = (const u16*)d_in[2];
  const u16* wv = (const u16*)d_in[3];
  const u16* wo = (const u16*)d_in[4];
  const u16* w1 = (const u16*)d_in[6];
  const u16* w2 = (const u16*)d_in[7];
  const u16* w3 = (const u16*)d_in[8];
  const u16* w4 = (const u16*)d_in[9];
  u16* out = (u16*)d_out;
  char* ws = (char*)d_ws;
  size_t off = 0;
  auto take = [&](size_t bytes) { char* p = ws + off; off += (bytes + 4095) & ~(size_t)4095; return p; };
  u16* Qb = (u16*)take((size_t)S * D * 2);
  char* KVb = take((size_t)64 * KV_TILE + 1024);
  u16* Ob = (u16*)take((size_t)S * D * 2);
  u16* hb = (u16*)take((size_t)S * D * 2);
  u16* hhb = (u16*)take((size_t)S * D * 2);
  u16* x1b = (u16*)take((size_t)S * D * 2);
  u16* gb = (u16*)take((size_t)S * F * 2);
  u16* wop = (u16*)take((size_t)D * D * 2);
  u16* w1p = (u16*)take((size_t)D * D * 2);
  u16* w2p = (u16*)take((size_t)D * D * 2);
  u16* w3p = (u16*)take((size_t)D * F * 2);
  u16* w4p = (u16*)take((size_t)D * F * 2);
  float* sth = (float*)take((size_t)S * 32 * 4);
  float* stx = (float*)take((size_t)S * 32 * 4);
  float* kmx = (float*)take(1024);
  u16* xp = (u16*)take((size_t)S * D * 2);
  u16* wqp = (u16*)take((size_t)D * 688 * 2);
  float* st0 = (float*)take((size_t)S * 32 * 4);

  pack_kernel<<<(PW_D + 3) / 4, 256, 0, stream>>>(x, wq, wk, wv, wo, w1, w2, w3, w4, xp, st0, wqp, wop, w1p, w2p, w3p, w4p);
  gemm_kernel<EPI_QKV><<<256, 768, 0, stream>>>(xp, wqp, nullptr, nullptr, st0, kmx, Qb, KVb);
  attn_kernel<<<512, 512, 0, stream>>>(Qb, KVb, kmx, Ob);
  gemm_kernel<EPI_WO><<<256, 768, 0, stream>>>(Ob, wop, x, nullptr, nullptr, sth, hb, nullptr);
  gemm_kernel<EPI_W1><<<256, 768, 0, stream>>>(hb, w1p, nullptr, nullptr, sth, nullptr, hhb, nullptr);
  gemm_kernel<EPI_W2><<<256, 768, 0, stream>>>(hhb, w2p, hb, nullptr, sth, stx, x1b, nullptr);
  gemm_kernel<EPI_W3><<<256, 768, 0, stream>>>(x1b, w3p, nullptr, nullptr, stx, nullptr, gb, nullptr);
  gemm_kernel<EPI_W4><<<256, 768, 0, stream>>>(gb, w4p, hb, x1b, nullptr, nullptr, out, nullptr);
}
```

```cpp
#include <hip/hip_runtime.h>
#include <hip/hip_bf16.h>
#include <stdint.h>
#include <type_traits>

typedef __attribute__((ext_vector_type(8))) __bf16 bf16x8;
typedef __attribute__((ext_vector_type(2))) __bf16 bf16x2;
typedef __attribute__((ext_vector_type(4))) float f32x4;
typedef __attribute__((ext_vector_type(2))) float f32x2;
typedef __attribute__((ext_vector_type(16))) float f32x16;
typedef __attribute__((ext_vector_type(4))) unsigned u32x4;
typedef __attribute__((ext_vector_type(2))) unsigned u32x2;
typedef short v4i16 __attribute__((ext_vector_type(4)));
typedef unsigned short u16;
#define LAS __attribute__((address_space(3)))
#define DI __device__ __forceinline__

constexpr int S = 2048, D = 640, H = 32, DH = 20, F = 1728;
constexpr float LN_EPS = 1e-5f;
constexpr float QSCALE = 0.22360679774997896f * 1.4426950408889634f;
constexpr int KV_TILE = 2880;
constexpr int KV_K1 = 1024, KV_V = 1536;

DI float bf2f(unsigned u) { return __uint_as_float(u << 16); }
DI float bflo(unsigned u) { return __uint_as_float(u << 16); }
DI float bfhi(unsigned u) { return __uint_as_float(u & 0xffff0000u); }
DI unsigned pack2(float a, float b) { f32x2 f = {a, b}; bf16x2 c = __builtin_convertvector(f, bf16x2); return __builtin_bit_cast(unsigned, c); }
DI float rbf(float a) { return bflo(pack2(a, 0.f)); }
DI float gelu_t(float x) {
  const float c0 = -2.f * 0.7978845608028654f * 1.4426950408889634f;
  float u = x * __builtin_fmaf(x * x, 0.044715f, 1.f);
  float e = __builtin_amdgcn_exp2f(u * c0);
  return x * __builtin_amdgcn_rcpf(1.f + e);
}
#define MFMA16(a, b, c) __builtin_amdgcn_mfma_f32_16x16x32_bf16((a), (b), (c), 0, 0, 0)
#define MFMA32(a, b, c) __builtin_amdgcn_mfma_f32_32x32x16_bf16((a), (b), (c), 0, 0, 0)

DI void glds16(const void* g, unsigned lds_base) {
  unsigned sv;
  asm volatile("s_mov_b32 %0, m0\n\ts_mov_b32 m0, %2\n\ts_nop 0\n\tglobal_load_lds_dwordx4 %1, off\n\ts_mov_b32 m0, %0"
               : "=&s"(sv) : "v"(g), "s"(lds_base) : "memory");
}
DI unsigned lds_addr(const void* p) { return (unsigned)(size_t)(const LAS char*)p; }

DI int apack_off(int row, int col, int KS) {
  return (((row >> 4) * KS + (col >> 5)) * 64 + (row & 15) + 16 * ((col >> 3) & 3)) * 8 + (col & 7);
}

constexpr int PW_X = 512;
constexpr int WT_KV = 100, WT_A = 120, WT_B = WT_A + 300, WT_C = WT_B + 280, WT_END = WT_C + 270;

DI void pack_wtile(int id, char* wl, const u16* __restrict__ wq, const u16* __restrict__ wk, const u16* __restrict__ wv,
                   const u16* __restrict__ wo, const u16* __restrict__ w1, const u16* __restrict__ w2,
                   const u16* __restrict__ w3, const u16* __restrict__ w4, u16* __restrict__ wqp, u16* __restrict__ wop,
                   u16* __restrict__ w1p, u16* __restrict__ w2p, u16* __restrict__ w3p, u16* __restrict__ w4p) {
  const int lane = threadIdx.x & 63;
  const int blk = lane >> 3, no2 = lane & 1, kg = (lane >> 1) & 3;
  const u16* W; u16* P; int N, KS, ks, nb0, nblk = 8;
  bool kvt = false;
  if (id < WT_KV) { W = wq; P = wqp; N = D; KS = 20; ks = id / 5; nb0 = (id % 5) * 8; }
  else if (id < WT_A) { W = wk; P = wqp; N = DH; KS = 20; ks = id - WT_KV; nb0 = 40; nblk = 3; kvt = true; }
  else if (id < WT_B) {
    const int u = id - WT_A, which = u / 100, uu = u % 100;
    W = which == 0 ? wo : which == 1 ? w1 : w2;
    P = which == 0 ? wop : which == 1 ? w1p : w2p;
    N = D; KS = 20; ks = uu / 5; nb0 = (uu % 5) * 8;
  } else if (id < WT_C) {
    const int u = id - WT_B; W = w3; P = w3p; N = F; KS = 20; ks = u / 14; nb0 = (u % 14) * 8; if (nb0 == 104) nblk = 4;
  } else {
    const int u = id - WT_C; W = w4; P = w4p; N = D; KS = 54; ks = u / 5; nb0 = (u % 5) * 8;
  }
  u32x4 r[8];
  if (!kvt) {
    const int bb = blk < nblk ? blk : 0;
    const u16* src = W + (size_t)(ks * 32 + kg * 8) * N + (nb0 + bb) * 16 + no2 * 8;
#pragma unroll
    for (int i = 0; i < 8; ++i) r[i] = *(const u32x4*)(src + (size_t)i * N);
  } else {
    const u16* sp[2]; unsigned msk[2];
#pragma unroll
    for (int h2 = 0; h2 < 2; ++h2) {
      const int c4 = blk * 4 + no2 * 2 + h2;
      sp[h2] = (c4 < 5 ? wk + 4 * c4 : wv + 4 * (c4 < 10 ? c4 - 5 : 0)) + (size_t)(ks * 32 + kg * 8) * DH;
      msk[h2] = c4 < 10 ? 0xffffffffu : 0u;
    }
#pragma unroll
    for (int i = 0; i < 8; ++i) {
#pragma unroll
      for (int h2 = 0; h2 < 2; ++h2) {
        const u32x2 e = *(const u32x2*)(sp[h2] + i * DH);
        r[i][2 * h2] = e[0] & msk[h2]; r[i][2 * h2 + 1] = e[1] & msk[h2];
      }
    }
  }
  {
    char* d = wl + blk * 1152 + (no2 + 2 * kg) * 144;
#pragma unroll
    for (int i = 0; i < 8; ++i) {
      u32x4 o;
#pragma unroll
      for (int p = 0; p < 4; ++p)
        o[p] = __builtin_amdgcn_perm(r[2 * p + 1][i >> 1], r[2 * p][i >> 1], (i & 1) ? 0x07060302u : 0x05040100u);
      *(u32x4*)(d + i * 16) = o;
    }
  }
  asm volatile("s_waitcnt lgkmcnt(0)" ::: "memory");
#pragma unroll
  for (int bq = 0; bq < 8; ++bq) {
    if (bq < nblk) {
      const u32x4 o = *(const u32x4*)(wl + bq * 1152 + (lane >> 3) * 144 + (lane & 7) * 16);
      *(u32x4*)(P + ((size_t)((nb0 + bq) * KS + ks) * 64 + lane) * 8) = o;
    }
  }
}

__global__ __launch_bounds__(256) void pack_kernel(const u16* __restrict__ x, const u16* __restrict__ wq, const u16* __restrict__ wk,
                                                   const u16* __restrict__ wv, u16* __restrict__ xp, float* __restrict__ stx,
                                                   u16* __restrict__ wqp) {
  __shared__ __attribute__((aligned(16))) char lds[4 * 8 * 1152];
  const int lane = threadIdx.x & 63;
  const int wv_ = __builtin_amdgcn_readfirstlane(blockIdx.x * 4 + (threadIdx.x >> 6));
  if (wv_ < PW_X) {
    const int mb = wv_ >> 2, kq = wv_ & 3, r16 = lane & 15, g = lane >> 4;
    u32x4 v[5];
#pragma unroll
    for (int i = 0; i < 5; ++i) v[i] = *(const u32x4*)(x + (size_t)(mb * 16 + r16) * D + (kq * 5 + i) * 32 + g * 8);
    float s1 = 0.f, s2 = 0.f;
#pragma unroll
    for (int i = 0; i < 5; ++i) {
      *(u32x4*)(xp + ((size_t)(mb * 20 + kq * 5 + i) * 64 + lane) * 8) = v[i];
#pragma unroll
      for (int j = 0; j < 4; ++j) { const float e0 = bflo(v[i][j]), e1 = bfhi(v[i][j]); s1 += e0 + e1; s2 += e0 * e0 + e1 * e1; }
    }
    s1 += __shfl_xor(s1, 16); s2 += __shfl_xor(s2, 16);
    s1 += __shfl_xor(s1, 32); s2 += __shfl_xor(s2, 32);
    *(f32x2*)(stx + (size_t)(mb * 16 + r16) * 32 + (kq + 4 * g) * 2) = (g == 0) ? (f32x2){s1, s2} : (f32x2){0.f, 0.f};
    return;
  }
  if (wv_ < PW_X + WT_A)
    pack_wtile(wv_ - PW_X, lds + (threadIdx.x >> 6) * (8 * 1152), wq, wk, wv, nullptr, nullptr, nullptr, nullptr, nullptr, wqp,
               nullptr, nullptr, nullptr, nullptr, nullptr);
}

constexpr int ACH = 4 * KV_TILE;
constexpr float ATHR = 16.f;
#define SB() __builtin_amdgcn_sched_barrier(0)
#define EXP2(x) __builtin_amdgcn_exp2f(x)

template <bool ONLINE>
DI void attn_pass(const char* __restrict__ KV, char* lds, int lane, int wi, int half, const u32x4 (&q0u)[2], u32x4 (&q1u)[2],
                  f32x16 (&O)[2], float (&mcur)[2]) {
  const int hi = lane >> 5, l32 = lane & 31;
  const unsigned lds0 = lds_addr(lds);
  auto stage = [&](int c, int buf) {
    const char* src = KV + (size_t)(half * 32 + c * 4) * KV_TILE + lane * 16;
    const unsigned dst = __builtin_amdgcn_readfirstlane(lds0 + (buf * 2 + half) * ACH + wi * 1024);
    glds16(src + wi * 1024, dst);
    glds16(src + (wi + 4) * 1024, dst + 4096);
    if (wi < 3 || lane < 16) glds16(src + (wi + 8) * 1024, dst + 8192);
  };
  stage(0, 0); stage(1, 1);
  f32x16 Z;
#pragma unroll
  for (int i = 0; i < 16; ++i) { O[0][i] = 0.f; O[1][i] = 0.f; Z[i] = 0.f; }
  const int koff = lane * 16, k1off = KV_K1 + l32 * 16;
  const int voff = KV_V + (hi * 21 + (l32 < 20 ? l32 : 20)) * 16;
  asm volatile("s_waitcnt vmcnt(0)" ::: "memory");
  __syncthreads();
  const char* hb = lds + half * ACH;
  f32x16 Sx[2];
  bf16x8 k0n, k1n, v0, v1;
  {
    const bf16x8 k0 = *(const bf16x8*)(hb + koff), k1 = *(const bf16x8*)(hb + k1off);
#pragma unroll
    for (int u = 0; u < 2; ++u) {
      Sx[u] = MFMA32(k0, __builtin_bit_cast(bf16x8, q0u[u]), Z);
      Sx[u] = MFMA32(k1, __builtin_bit_cast(bf16x8, q1u[u]), Sx[u]);
    }
    k0n = *(const bf16x8*)(hb + KV_TILE + koff); k1n = *(const bf16x8*)(hb + KV_TILE + k1off);
    v0 = *(const bf16x8*)(hb + voff); v1 = *(const bf16x8*)(hb + voff + 672);
    if (ONLINE) {
#pragma unroll
      for (int u = 0; u < 2; ++u) {
        float mx = fmaxf(fmaxf(Sx[u][0], Sx[u][1]), Sx[u][2]);
#pragma unroll
        for (int i = 3; i < 15; i += 2) mx = fmaxf(fmaxf(mx, Sx[u][i]), Sx[u][i + 1]);
        mx = fmaxf(mx, Sx[u][15]);
        const float mo = fmaxf(mx, __shfl_xor(mx, 32));
        mcur[u] = rbf(mo);
#pragma unroll
        for (int i = 0; i < 16; ++i) Sx[u][i] -= mcur[u];
        q1u[u][2] = hi ? 0u : (pack2(-mcur[u], 0.f) & 0xffffu);
      }
    }
  }
  int bcur = 0;
  for (int c = 0; c < 8; ++c) {
    const int bn1 = (bcur == 2) ? 0 : bcur + 1, bn2 = (bn1 == 2) ? 0 : bn1 + 1;
    if (c + 2 < 8) stage(c + 2, bn2);
    const char* cb = hb + bcur * (2 * ACH);
    const char* nb = hb + bn1 * (2 * ACH);
#pragma unroll
    for (int t = 0; t < 4; ++t) {
      const char* tv = (t < 3) ? cb + (t + 1) * KV_TILE : nb;
      const char* tk = (t < 2) ? cb + (t + 2) * KV_TILE : nb + (t - 2) * KV_TILE;
      f32x16 Sn[2];
#pragma unroll
      for (int u = 0; u < 2; ++u) {
        u32x4 p0, p1;
        SB();
        Sn[u] = MFMA32(k0n, __builtin_bit_cast(bf16x8, q0u[u]), Z);
        SB();
        p0[0] = pack2(EXP2(Sx[u][0]), EXP2(Sx[u][1])); p0[1] = pack2(EXP2(Sx[u][2]), EXP2(Sx[u][3]));
        SB();
        Sn[u] = MFMA32(k1n, __builtin_bit_cast(bf16x8, q1u[u]), Sn[u]);
        if (u == 1) { k0n = *(const bf16x8*)(tk + koff); k1n = *(const bf16x8*)(tk + k1off); }
        SB();
        p0[2] = pack2(EXP2(Sx[u][4]), EXP2(Sx[u][5])); p0[3] = pack2(EXP2(Sx[u][6]), EXP2(Sx[u][7]));
        SB();
        O[u] = MFMA32(v0, __builtin_bit_cast(bf16x8, p0), O[u]);
        if (u == 1) v0 = *(const bf16x8*)(tv + voff);
        SB();
        p1[0] = pack2(EXP2(Sx[u][8]), EXP2(Sx[u][9])); p1[1] = pack2(EXP2(Sx[u][10]), EXP2(Sx[u][11]));
        p1[2] = pack2(EXP2(Sx[u][12]), EXP2(Sx[u][13])); p1[3] = pack2(EXP2(Sx[u][14]), EXP2(Sx[u][15]));
        SB();
        O[u] = MFMA32(v1, __builtin_bit_cast(bf16x8, p1), O[u]);
        if (u == 1) v1 = *(const bf16x8*)(tv + voff + 672);
        SB();
      }
#pragma unroll
      for (int u = 0; u < 2; ++u) {
        if (ONLINE) {
          float mx = fmaxf(fmaxf(Sn[u][0], Sn[u][1]), Sn[u][2]);
#pragma unroll
          for (int i = 3; i < 15; i += 2) mx = fmaxf(fmaxf(mx, Sn[u][i]), Sn[u][i + 1]);
          mx = fmaxf(mx, Sn[u][15]);
          if (!(c == 7 && t == 3) && __any(mx > ATHR)) {
            const float mo = fmaxf(mx, __shfl_xor(mx, 32));
            const float mnew = rbf(mcur[u] + fmaxf(mo, 0.f));
            const float delta = mnew - mcur[u];
            const float sc = EXP2(-delta);
#pragma unroll
            for (int i = 0; i < 16; ++i) { Sn[u][i] -= delta; O[u][i] *= sc; }
            mcur[u] = mnew;
            q1u[u][2] = hi ? 0u : (pack2(-mnew, 0.f) & 0xffffu);
          }
        }
        Sx[u] = Sn[u];
      }
    }
    asm volatile("s_waitcnt vmcnt(0)" ::: "memory");
    __syncthreads();
    bcur = bn1;
  }
}

__global__ __launch_bounds__(512, 2) void attn_kernel(const u16* __restrict__ Q, const char* __restrict__ KV,
                                                      const float* __restrict__ kmax, u16* __restrict__ Op,
                                                      const u16* __restrict__ wo, const u16* __restrict__ w1,
                                                      const u16* __restrict__ w2, const u16* __restrict__ w3,
                                                      const u16* __restrict__ w4, u16* __restrict__ wop, u16* __restrict__ w1p,
                                                      u16* __restrict__ w2p, u16* __restrict__ w3p, u16* __restrict__ w4p) {
  __shared__ __attribute__((aligned(16))) char lds[8 * 8 * 1152];
  const int tid = threadIdx.x, lane = tid & 63;
  const int w = __builtin_amdgcn_readfirstlane(tid >> 6);
  if (blockIdx.x >= 256) {
    const int id = WT_A + (blockIdx.x - 256) * 8 + w;
    if (id < WT_END)
      pack_wtile(id, lds + w * (8 * 1152), nullptr, nullptr, nullptr, wo, w1, w2, w3, w4, nullptr, wop, w1p, w2p, w3p, w4p);
    return;
  }
  const int hi = lane >> 5, l32 = lane & 31;
  const int b = blockIdx.x, xc = b & 7, bi = b >> 3;
  const int tb = xc * 8 + (bi >> 2), hg = bi & 3;
  const int head0 = hg * 8 + (w & 3) * 2, half = w >> 2, wi = w & 3;
  const int tok = tb * 32 + l32;
  float kk;
  {
    const f32x2 ka = *(const f32x2*)(kmax + lane * 2), kb = *(const f32x2*)(kmax + 128 + lane * 2);
    float ma = fmaxf(ka[0], ka[1]), mb = fmaxf(kb[0], kb[1]);
#pragma unroll
    for (int o = 1; o < 64; o <<= 1) { ma = fmaxf(ma, __shfl_xor(ma, o)); mb = fmaxf(mb, __shfl_xor(mb, o)); }
    kk = ma + mb;
  }
  u32x4 q0u[2], q1u[2];
  float mcur[2];
#pragma unroll
  for (int u = 0; u < 2; ++u) {
    const u16* qp = Q + (size_t)tok * D + (head0 + u) * DH;
    const u32x2 a = *(const u32x2*)(qp + 8 * hi), c = *(const u32x2*)(qp + 8 * hi + 4);
    q0u[u] = (u32x4){a[0], a[1], c[0], c[1]};
    u32x2 e = *(const u32x2*)(qp + 16);
    q1u[u] = (u32x4){hi ? 0u : e[0], hi ? 0u : e[1], 0u, 0u};
    float qq = 0.f;
#pragma unroll
    for (int i = 0; i < 4; ++i) { qq += bflo(q0u[u][i]) * bflo(q0u[u][i]) + bfhi(q0u[u][i]) * bfhi(q0u[u][i]); }
#pragma unroll
    for (int i = 0; i < 2; ++i) { qq += bflo(q1u[u][i]) * bflo(q1u[u][i]) + bfhi(q1u[u][i]) * bfhi(q1u[u][i]); }
    qq += __shfl_xor(qq, 32);
    mcur[u] = rbf(__builtin_sqrtf(qq * kk) * 1.02f + 1e-30f);
    q1u[u][2] = hi ? 0u : (pack2(-mcur[u], 0.f) & 0xffffu);
  }
  f32x16 O[2];
  attn_pass<false>(KV, lds, lane, wi, half, q0u, q1u, O, mcur);
  {
    const float l0 = hi ? O[0][8] : O[0][12], l1 = hi ? O[1][8] : O[1][12];
    const int bad = !(l0 >= 8.6736174e-19f && l0 < 1e30f && l1 >= 8.6736174e-19f && l1 < 1e30f);
    if (__syncthreads_or(bad)) {
      q1u[0][2] = 0u; q1u[1][2] = 0u;
      attn_pass<true>(KV, lds, lane, wi, half, q0u, q1u, O, mcur);
    }
  }
  float* cx = (float*)lds;
  if (half == 1) {
#pragma unroll
    for (int u = 0; u < 2; ++u) {
#pragma unroll
      for (int i = 0; i < 16; ++i) cx[((wi * 2 + u) * 17 + i) * 64 + lane] = O[u][i];
      cx[((wi * 2 + u) * 17 + 16) * 64 + lane] = mcur[u];
    }
  }
  __syncthreads();
  if (half == 0) {
#pragma unroll
    for (int u = 0; u < 2; ++u) {
      const float m2 = cx[((wi * 2 + u) * 17 + 16) * 64 + lane];
      const float mm = fmaxf(mcur[u], m2);
      const float f1 = EXP2(mcur[u] - mm), f2 = EXP2(m2 - mm);
#pragma unroll
      for (int i = 0; i < 16; ++i) O[u][i] = O[u][i] * f1 + cx[((wi * 2 + u) * 17 + i) * 64 + lane] * f2;
      const float l = hi ? O[u][8] : O[u][12];
      const float rl = 1.f / l;
      const int col0 = (head0 + u) * DH + 4 * hi;
#pragma unroll
      for (int q = 0; q < 3; ++q) {
        if (q < 2 || hi == 0) {
          u32x2 o = {pack2(O[u][4 * q] * rl, O[u][4 * q + 1] * rl), pack2(O[u][4 * q + 2] * rl, O[u][4 * q + 3] * rl)};
          *(u32x2*)(Op + apack_off(tok, col0 + 8 * q, 20)) = o;
        }
      }
    }
  }
}

enum { EPI_WO = 0, EPI_W1 = 1, EPI_W2 = 2, EPI_W3 = 3, EPI_W4 = 4, EPI_QKV = 5 };

template <int N> DI void wait_vm_barrier() {
  asm volatile("s_waitcnt vmcnt(%0)\n\ts_barrier" :: "i"(N) : "memory");
}

template <int I, int CNT> DI void drain_tail() {
  wait_vm_barrier<I * CNT>();
  if constexpr (I > 0) drain_tail<I - 1, CNT>();
}

template <int EPI> struct GCfg {
  static constexpr int MPW = (EPI == EPI_W3) ? 2 : 1;
  static constexpr int NBMAX = (EPI == EPI_W3) ? 7 : (EPI == EPI_QKV) ? 6 : 5;
  static constexpr int PRE = (EPI == EPI_W3) ? 2 : 3;
  static constexpr int LDSB = (PRE + 1) * 2 * (4 * MPW + NBMAX) * 1024 + 1024;
};

template <int EPI, int NB, int NS, int NC>
DI void gemm_compute(char* lds, const u16* __restrict__ r1, const u16* __restrict__ r2, const float* __restrict__ stin,
                     float* __restrict__ stout, u16* __restrict__ out, char* __restrict__ KVo, int w, int mblk0, int nblk0,
                     int nbcnt, int nt) {
  constexpr bool LN = (EPI == EPI_W1 || EPI == EPI_W3 || EPI == EPI_QKV);
  constexpr bool NEEDST = (EPI == EPI_W1 || EPI == EPI_W2 || EPI == EPI_W3 || EPI == EPI_QKV);
  constexpr int MPW = GCfg<EPI>::MPW, MBT = 4 * MPW;
  constexpr int KST = (EPI == EPI_W4) ? 54 : 20, NST = KST / 2;
  constexpr int PPK = MBT + NB, RING = GCfg<EPI>::PRE + 1, SLOT = 2 * PPK * 1024;
  const int lane = threadIdx.x & 63, g = lane >> 4, r16 = lane & 15;
  const int wm = w & 3;

  f32x4 stv[MPW][2];
  u32x2 ra[MPW][NC], rb[MPW][NC];
#pragma unroll
  for (int o = 0; o < MPW; ++o) {
    const int tok = (mblk0 + wm * MPW + o) * 16 + r16;
    if (NEEDST) {
#pragma unroll
      for (int q = 0; q < 2; ++q) stv[o][q] = *(const f32x4*)(stin + (size_t)(tok - r16) * 32 + q * 256 + lane * 4);
    }
#pragma unroll
    for (int nb = 0; nb < NC; ++nb) {
      const int col = (nblk0 + NS + nb) * 16 + 4 * g;
      if (EPI == EPI_WO) ra[o][nb] = *(const u32x2*)(r1 + (size_t)tok * D + col);
      if (EPI == EPI_W2 || EPI == EPI_W4) ra[o][nb] = *(const u32x2*)(r1 + apack_off(tok, col, 20));
      if (EPI == EPI_W4) rb[o][nb] = *(const u32x2*)(r2 + apack_off(tok, col, 20));
    }
  }
  __builtin_amdgcn_sched_barrier(0);

  f32x4 acc[MPW][NC], cs = {0.f, 0.f, 0.f, 0.f};
#pragma unroll
  for (int nb = 0; nb < NC; ++nb)
#pragma unroll
    for (int o = 0; o < MPW; ++o) acc[o][nb] = (f32x4){0.f, 0.f, 0.f, 0.f};
  const u32x4 ones4 = {0x3F803F80u, 0x3F803F80u, 0x3F803F80u, 0x3F803F80u};
  const bf16x8 ones = __builtin_bit_cast(bf16x8, ones4);
  const int csj = MBT + (w < NB ? w : 0);

  int slot = 0;
  for (int s = 0; s < NST; ++s) {
    asm volatile("s_waitcnt lgkmcnt(0)\n\ts_barrier" ::: "memory");
    const char* sb = lds + slot * SLOT + lane * 16;
    slot = (slot == RING - 1) ? 0 : slot + 1;
    bf16x8 af[2][MPW], bfr[2][NC], cf[2];
#pragma unroll
    for (int kl = 0; kl < 2; ++kl) {
#pragma unroll
      for (int o = 0; o < MPW; ++o) af[kl][o] = *(const bf16x8*)(sb + (kl * PPK + wm * MPW + o) * 1024);
#pragma unroll
      for (int nb = 0; nb < NC; ++nb) bfr[kl][nb] = *(const bf16x8*)(sb + (kl * PPK + MBT + NS + nb) * 1024);
      if (LN) cf[kl] = *(const bf16x8*)(sb + (kl * PPK + csj) * 1024);
    }
    __builtin_amdgcn_sched_barrier(0);
#pragma unroll
    for (int kl = 0; kl < 2; ++kl) {
#pragma unroll
      for (int nb = 0; nb < NC; ++nb)
#pragma unroll
        for (int o = 0; o < MPW; ++o) acc[o][nb] = MFMA16(bfr[kl][nb], af[kl][o], acc[o][nb]);
      if (LN) cs = MFMA16(cf[kl], ones, cs);
    }
  }
  f32x4 c[NC];
  if (LN) {
    float* csl = (float*)(lds + GCfg<EPI>::LDSB - 1024);
    if (r16 == 0 && w < NB) *(f32x4*)(csl + w * 16 + 4 * g) = cs;
    __syncthreads();
#pragma unroll
    for (int nb = 0; nb < NC; ++nb) c[nb] = *(const f32x4*)(csl + (NS + nb) * 16 + 4 * g);
  }

#pragma unroll
  for (int o = 0; o < MPW; ++o) {
    const int tok = (mblk0 + wm * MPW + o) * 16 + r16;
    float mean = 0.f, rs = 0.f;
    if (NEEDST) {
      float t1 = 0.f, t2 = 0.f;
#pragma unroll
      for (int q = 0; q < 2; ++q) {
        float u1 = stv[o][q][0] + stv[o][q][2], u2 = stv[o][q][1] + stv[o][q][3];
#pragma unroll
        for (int of = 1; of < 8; of <<= 1) { u1 += __shfl_xor(u1, of); u2 += __shfl_xor(u2, of); }
        const float v1 = __shfl(u1, (r16 & 7) * 8), v2 = __shfl(u2, (r16 & 7) * 8);
        if ((r16 >> 3) == q) { t1 = v1; t2 = v2; }
      }
      mean = t1 * (1.f / D);
      const float var = fmaxf(t2 * (1.f / D) - mean * mean, 0.f);
      rs = __builtin_amdgcn_rsqf(var + LN_EPS);
      if (EPI == EPI_W1) rs = rs * __builtin_amdgcn_rsqf(var * rs * rs + LN_EPS);
    }
    float q1 = 0.f, q2 = 0.f;
#pragma unroll
    for (int nb = 0; nb < NC; ++nb) {
      const int col = (nblk0 + NS + nb) * 16 + 4 * g;
      float z[4];
      if (EPI == EPI_WO) {
        const float xr[4] = {bflo(ra[o][nb][0]), bfhi(ra[o][nb][0]), bflo(ra[o][nb][1]), bfhi(ra[o][nb][1])};
#pragma unroll
        for (int r = 0; r < 4; ++r) { z[r] = rbf(xr[r] + rbf(acc[o][nb][r])); q1 += z[r]; q2 += z[r] * z[r]; }
        u32x2 ov = {pack2(z[0], z[1]), pack2(z[2], z[3])};
        *(u32x2*)(out + apack_off(tok, col, 20)) = ov;
      } else if (EPI == EPI_W1) {
#pragma unroll
        for (int r = 0; r < 4; ++r) z[r] = gelu_t(rbf((acc[o][nb][r] - mean * c[nb][r]) * rs));
        u32x2 ov = {pack2(z[0], z[1]), pack2(z[2], z[3])};
        const int grp = nt >> 1;
        const int sh = (grp == 0) ? 1 : (grp == 1) ? -1 : (grp == 2) ? 2 : -2;
        const int t2 = tok + sh;
        if (t2 >= 0 && t2 < S) *(u32x2*)(out + apack_off(t2, col, 20)) = ov;
        if (tok - sh < 0 || tok - sh >= S) *(u32x2*)(out + apack_off(tok, col, 20)) = (u32x2){0u, 0u};
      } else if (EPI == EPI_W2) {
        const float hr[4] = {bflo(ra[o][nb][0]), bfhi(ra[o][nb][0]), bflo(ra[o][nb][1]), bfhi(ra[o][nb][1])};
#pragma unroll
        for (int r = 0; r < 4; ++r) { z[r] = rbf(rbf((hr[r] - mean) * rs) + rbf(acc[o][nb][r])); q1 += z[r]; q2 += z[r] * z[r]; }
        u32x2 ov = {pack2(z[0], z[1]), pack2(z[2], z[3])};
        *(u32x2*)(out + apack_off(tok, col, 20)) = ov;
      } else if (EPI == EPI_W3) {
#pragma unroll
        for (int r = 0; r < 4; ++r) z[r] = gelu_t(rbf((acc[o][nb][r] - mean * c[nb][r]) * rs));
        u32x2 ov = {pack2(z[0], z[1]), pack2(z[2], z[3])};
        if (NS + nb < nbcnt) *(u32x2*)(out + apack_off(tok, col, 54)) = ov;
      } else if (EPI == EPI_QKV) {
        if (NS + nb < 5) {
#pragma unroll
          for (int r = 0; r < 4; ++r) z[r] = (acc[o][nb][r] - mean * c[nb][r]) * (rs * QSCALE);
          u32x2 ov = {pack2(z[0], z[1]), pack2(z[2], z[3])};
          *(u32x2*)(out + (size_t)tok * D + col) = ov;
        } else {
#pragma unroll
          for (int r = 0; r < 4; ++r) z[r] = (acc[o][nb][r] - mean * c[nb][r]) * rs;
          const int c4 = nt * 4 + g;
          if (nt < 2) {
            float nn = 0.f;
            if (c4 < 5) {
#pragma unroll
              for (int r = 0; r < 4; ++r) { const float t = rbf(z[r]); nn += t * t; }
            }
            nn += __shfl_xor(nn, 16); nn += __shfl_xor(nn, 32);
#pragma unroll
            for (int of = 1; of < 16; of <<= 1) nn = fmaxf(nn, __shfl_xor(nn, of));
            if (lane == 0) stout[nt * 128 + (tok >> 4)] = nn;
          }
          char* tb = KVo + (size_t)(tok >> 5) * KV_TILE;
          const int key = tok & 31;
          if (c4 < 4) {
            u32x2 ov = {pack2(z[0], z[1]), pack2(z[2], z[3])};
            *(u32x2*)(tb + (key + 32 * (c4 >> 1)) * 16 + (c4 & 1) * 8) = ov;
          } else if (c4 == 4) {
            *(u32x4*)(tb + KV_K1 + key * 16) = (u32x4){pack2(z[0], z[1]), pack2(z[2], z[3]), 0x00003F80u, 0u};
          } else if (c4 < 10) {
            const int sk = key >> 4, hh = (key >> 2) & 1, j = ((key >> 3) & 1) * 4 + (key & 3);
            u16* vb = (u16*)(tb + KV_V) + ((sk * 2 + hh) * 21 + 4 * (c4 - 5)) * 8 + j;
#pragma unroll
            for (int r = 0; r < 4; ++r) vb[r * 8] = (u16)(pack2(z[r], 0.f) & 0xffffu);
            if (c4 == 9) vb[4 * 8] = (u16)0x3F80;
          }
        }
      } else {
        const float hr[4] = {bflo(ra[o][nb][0]), bfhi(ra[o][nb][0]), bflo(ra[o][nb][1]), bfhi(ra[o][nb][1])};
        const float xr[4] = {bflo(rb[o][nb][0]), bfhi(rb[o][nb][0]), bflo(rb[o][nb][1]), bfhi(rb[o][nb][1])};
#pragma unroll
        for (int r = 0; r < 4; ++r) z[r] = hr[r] + rbf(xr[r] + rbf(acc[o][nb][r]));
        u32x2 ov = {pack2(z[0], z[1]), pack2(z[2], z[3])};
        *(u32x2*)(out + (size_t)tok * D + col) = ov;
      }
    }
    if (EPI == EPI_WO || EPI == EPI_W2) {
      q1 += __shfl_xor(q1, 16); q2 += __shfl_xor(q2, 16);
      q1 += __shfl_xor(q1, 32); q2 += __shfl_xor(q2, 32);
      if (lane < 16) *(f32x2*)(stout + (size_t)tok * 32 + (nt * 2 + (NS > 0 ? 1 : 0)) * 2) = (f32x2){q1, q2};
    }
  }
}

template <int EPI, int NB>
DI void gemm_body(char* lds, const u16* __restrict__ A, const u16* __restrict__ Bp, const u16* __restrict__ r1,
                  const u16* __restrict__ r2, const float* __restrict__ stin, float* __restrict__ stout, u16* __restrict__ out,
                  char* __restrict__ KVo) {
  constexpr int MPW = GCfg<EPI>::MPW, MBT = 4 * MPW;
  constexpr int KST = (EPI == EPI_W4) ? 54 : 20, NST = KST / 2;
  constexpr int PPK = MBT + NB;
  constexpr int PRE = GCfg<EPI>::PRE, RING = PRE + 1;
  constexpr int SLOT = 2 * PPK * 1024;
  const int lane = threadIdx.x & 63;
  const int w = __builtin_amdgcn_readfirstlane(threadIdx.x >> 6);
  const int b = blockIdx.x, xc = b & 7, bi = b >> 3;
  int mblk0, nblk0, nbcnt, nt;
  if (EPI == EPI_W3) {
    const int mt = xc * 2 + (bi >> 4); nt = bi & 15;
    mblk0 = mt * 8;
    nblk0 = nt < 12 ? 7 * nt : 84 + 6 * (nt - 12);
    nbcnt = nt < 12 ? 7 : 6;
  } else {
    const int mt = xc * 4 + (bi >> 3); nt = bi & 7;
    mblk0 = mt * 4; nblk0 = nt * 5; nbcnt = 5;
  }
  if (w >= 8) {
    const int kl = w & 1;
    const unsigned lds0 = lds_addr(lds) + kl * PPK * 1024;
    const u16* Ab = A + ((size_t)mblk0 * KST + kl) * 512 + lane * 8;
    const u16* Bb = Bp + ((size_t)nblk0 * KST + kl) * 512 + lane * 8;
    auto run = [&](auto J0c, auto CNTc) {
      constexpr int J0 = decltype(J0c)::value, CNT = decltype(CNTc)::value;
      auto issue = [&](int s, int slot) {
#pragma unroll
        for (int j = J0; j < J0 + CNT; ++j) {
          const unsigned dst = lds0 + slot * SLOT + j * 1024;
          if (j < MBT) glds16(Ab + ((size_t)j * KST + 2 * s) * 512, dst);
          else {
            const int nb = j - MBT;
            const int nn = (EPI == EPI_W3) ? (nb < nbcnt ? nb : nbcnt - 1) : (EPI == EPI_QKV && nb == 5) ? 40 + nt - nblk0 : nb;
            glds16(Bb + ((size_t)nn * KST + 2 * s) * 512, dst);
          }
        }
      };
#pragma unroll
      for (int s = 0; s < PRE; ++s) issue(s, s);
      int slot = PRE;
      for (int s = 0; s < NST - PRE; ++s) {
        wait_vm_barrier<(PRE - 1) * CNT>();
        issue(s + PRE, slot);
        slot = (slot == RING - 1) ? 0 : slot + 1;
      }
      drain_tail<PRE - 1, CNT>();
    };
    constexpr int H0 = (PPK + 1) / 2;
    if (w < 10) run(std::integral_constant<int, 0>{}, std::integral_constant<int, H0>{});
    else run(std::integral_constant<int, H0>{}, std::integral_constant<int, PPK - H0>{});
    return;
  }
  constexpr int N0 = (NB + 1) / 2;
  if (w < 4) gemm_compute<EPI, NB, 0, N0>(lds, r1, r2, stin, stout, out, KVo, w, mblk0, nblk0, nbcnt, nt);
  else gemm_compute<EPI, NB, N0, NB - N0>(lds, r1, r2, stin, stout, out, KVo, w, mblk0, nblk0, nbcnt, nt);
}

template <int EPI>
__global__ __launch_bounds__(768, 1) void gemm_kernel(const u16* __restrict__ A, const u16* __restrict__ Bp, const u16* __restrict__ r1,
                                                      const u16* __restrict__ r2, const float* __restrict__ stin,
                                                      float* __restrict__ stout, u16* __restrict__ out, char* __restrict__ KVo) {
  __shared__ __attribute__((aligned(16))) char lds[GCfg<EPI>::LDSB];
  if (EPI == EPI_QKV) {
    if (((blockIdx.x >> 3) & 7) < 3) gemm_body<EPI, 6>(lds, A, Bp, r1, r2, stin, stout, out, KVo);
    else gemm_body<EPI, 5>(lds, A, Bp, r1, r2, stin, stout, out, KVo);
  } else {
    gemm_body<EPI, GCfg<EPI>::NBMAX>(lds, A, Bp, r1, r2, stin, stout, out, KVo);
  }
}

extern "C" void kernel_launch(void* const* d_in, const int* in_sizes, int n_in, void* d_out, int out_size, void* d_ws,
                              size_t ws_size, hipStream_t stream) {
  const u16* x = (const u16*)d_in[0];
  const u16* wq = (const u16*)d_in[1];
  const u16* wk = (const u16*)d_in[2];
  const u16* wv = (const u16*)d_in[3];
  const u16* wo = (const u16*)d_in[4];
  const u16* w1 = (const u16*)d_in[6];
  const u16* w2 = (const u16*)d_in[7];
  const u16* w3 = (const u16*)d_in[8];
  const u16* w4 = (const u16*)d_in[9];
  u16* out = (u16*)d_out;
  char* ws = (char*)d_ws;
  size_t off = 0;
  auto take = [&](size_t bytes) { char* p = ws + off; off += (bytes + 4095) & ~(size_t)4095; return p; };
  u16* Qb = (u16*)take((size_t)S * D * 2);
  char* KVb = take((size_t)64 * KV_TILE + 1024);
  u16* Ob = (u16*)take((size_t)S * D * 2);
  u16* hb = (u16*)take((size_t)S * D * 2);
  u16* hhb = (u16*)take((size_t)S * D * 2);
  u16* x1b = (u16*)take((size_t)S * D * 2);
  u16* gb = (u16*)take((size_t)S * F * 2);
  u16* wop = (u16*)take((size_t)D * D * 2);
  u16* w1p = (u16*)take((size_t)D * D * 2);
  u16* w2p = (u16*)take((size_t)D * D * 2);
  u16* w3p = (u16*)take((size_t)D * F * 2);
  u16* w4p = (u16*)take((size_t)D * F * 2);
  float* sth = (float*)take((size_t)S * 32 * 4);
  float* stx = (float*)take((size_t)S * 32 * 4);
  float* kmx = (float*)take(1024);
  u16* xp = (u16*)take((size_t)S * D * 2);
  u16* wqp = (u16*)take((size_t)D * 688 * 2);
  float* st0 = (float*)take((size_t)S * 32 * 4);

  pack_kernel<<<(PW_X + WT_A + 3) / 4, 256, 0, stream>>>(x, wq, wk, wv, xp, st0, wqp);
  gemm_kernel<EPI_QKV><<<256, 768, 0, stream>>>(xp, wqp, nullptr, nullptr, st0, kmx, Qb, KVb);
  attn_kernel<<<256 + (WT_END - WT_A + 7) / 8, 512, 0, stream>>>(Qb, KVb, kmx, Ob, wo, w1, w2, w3, w4, wop, w1p, w2p, w3p, w4p);
  gemm_kernel<EPI_WO><<<256, 768, 0, stream>>>(Ob, wop, x, nullptr, nullptr, sth, hb, nullptr);
  gemm_kernel<EPI_W1><<<256, 768, 0, stream>>>(hb, w1p, nullptr, nullptr, sth, nullptr, hhb, nullptr);
  gemm_kernel<EPI_W2><<<256, 768, 0, stream>>>(hhb, w2p, hb, nullptr, sth, stx, x1b, nullptr);
  gemm_kernel<EPI_W3><<<256, 768, 0, stream>>>(x1b, w3p, nullptr, nullptr, stx, nullptr, gb, nullptr);
  gemm_kernel<EPI_W4><<<256, 768, 0, stream>>>(gb, w4p, hb, x1b, nullptr, nullptr, out, nullptr);
}
```

```cpp
#include <hip/hip_runtime.h>
#include <hip/hip_bf16.h>
#include <stdint.h>
#include <type_traits>

typedef __attribute__((ext_vector_type(8))) __bf16 bf16x8;
typedef __attribute__((ext_vector_type(2))) __bf16 bf16x2;
typedef __attribute__((ext_vector_type(4))) float f32x4;
typedef __attribute__((ext_vector_type(2))) float f32x2;
typedef __attribute__((ext_vector_type(16))) float f32x16;
typedef __attribute__((ext_vector_type(4))) unsigned u32x4;
typedef __attribute__((ext_vector_type(2))) unsigned u32x2;
typedef short v4i16 __attribute__((ext_vector_type(4)));
typedef unsigned short u16;
#define LAS __attribute__((address_space(3)))
#define DI __device__ __forceinline__

constexpr int S = 2048, D = 640, H = 32, DH = 20, F = 1728;
constexpr float LN_EPS = 1e-5f;
constexpr float QSCALE = 0.22360679774997896f * 1.4426950408889634f;
constexpr int KV_TILE = 2880;
constexpr int KV_K1 = 1024, KV_V = 1536;

DI float bf2f(unsigned u) { return __uint_as_float(u << 16); }
DI float bflo(unsigned u) { return __uint_as_float(u << 16); }
DI float bfhi(unsigned u) { return __uint_as_float(u & 0xffff0000u); }
DI unsigned pack2(float a, float b) { f32x2 f = {a, b}; bf16x2 c = __builtin_convertvector(f, bf16x2); return __builtin_bit_cast(unsigned, c); }
DI float rbf(float a) { return bflo(pack2(a, 0.f)); }
DI float gelu_t(float x) {
  const float c0 = -2.f * 0.7978845608028654f * 1.4426950408889634f;
  float u = x * __builtin_fmaf(x * x, 0.044715f, 1.f);
  float e = __builtin_amdgcn_exp2f(u * c0);
  return x * __builtin_amdgcn_rcpf(1.f + e);
}
#define MFMA16(a, b, c) __builtin_amdgcn_mfma_f32_16x16x32_bf16((a), (b), (c), 0, 0, 0)
#define MFMA32(a, b, c) __builtin_amdgcn_mfma_f32_32x32x16_bf16((a), (b), (c), 0, 0, 0)

DI void glds16(const void* g, unsigned lds_base) {
  unsigned sv;
  asm volatile("s_mov_b32 %0, m0\n\ts_mov_b32 m0, %2\n\ts_nop 0\n\tglobal_load_lds_dwordx4 %1, off\n\ts_mov_b32 m0, %0"
               : "=&s"(sv) : "v"(g), "s"(lds_base) : "memory");
}
DI unsigned lds_addr(const void* p) { return (unsigned)(size_t)(const LAS char*)p; }

DI int apack_off(int row, int col, int KS) {
  return (((row >> 4) * KS + (col >> 5)) * 64 + (row & 15) + 16 * ((col >> 3) & 3)) * 8 + (col & 7);
}

constexpr int PW_X = 512;
constexpr int PW_A = PW_X + 400;
constexpr int PW_B = PW_A + 280;
constexpr int PW_C = PW_B + 270;
constexpr int PW_D = PW_C + 20;

__global__ __launch_bounds__(256) void pack_kernel(const u16* __restrict__ x, const u16* __restrict__ wq, const u16* __restrict__ wk,
                                                   const u16* __restrict__ wv, const u16* __restrict__ wo, const u16* __restrict__ w1,
                                                   const u16* __restrict__ w2, const u16* __restrict__ w3, const u16* __restrict__ w4,
                                                   u16* __restrict__ xp, float* __restrict__ stx, u16* __restrict__ wqp,
                                                   u16* __restrict__ wop, u16* __restrict__ w1p, u16* __restrict__ w2p,
                                                   u16* __restrict__ w3p, u16* __restrict__ w4p) {
  __shared__ __attribute__((aligned(16))) char lds[4 * 8 * 1152];
  const int lane = threadIdx.x & 63;
  const int wv_ = __builtin_amdgcn_readfirstlane(blockIdx.x * 4 + (threadIdx.x >> 6));
  if (wv_ < PW_X) {
    const int mb = wv_ >> 2, kq = wv_ & 3, r16 = lane & 15, g = lane >> 4;
    u32x4 v[5];
#pragma unroll
    for (int i = 0; i < 5; ++i) v[i] = *(const u32x4*)(x + (size_t)(mb * 16 + r16) * D + (kq * 5 + i) * 32 + g * 8);
    float s1 = 0.f, s2 = 0.f;
#pragma unroll
    for (int i = 0; i < 5; ++i) {
      *(u32x4*)(xp + ((size_t)(mb * 20 + kq * 5 + i) * 64 + lane) * 8) = v[i];
#pragma unroll
      for (int j = 0; j < 4; ++j) { const float e0 = bflo(v[i][j]), e1 = bfhi(v[i][j]); s1 += e0 + e1; s2 += e0 * e0 + e1 * e1; }
    }
    s1 += __shfl_xor(s1, 16); s2 += __shfl_xor(s2, 16);
    s1 += __shfl_xor(s1, 32); s2 += __shfl_xor(s2, 32);
    *(f32x2*)(stx + (size_t)(mb * 16 + r16) * 32 + (kq + 4 * g) * 2) = (g == 0) ? (f32x2){s1, s2} : (f32x2){0.f, 0.f};
    return;
  }
  if (wv_ >= PW_D) return;
  const int blk = lane >> 3, no2 = lane & 1, kg = (lane >> 1) & 3;
  const u16* W; u16* P; int N, KS, ks, nb0, nblk = 8;
  bool kvt = false;
  if (wv_ < PW_A) {
    const int u = wv_ - PW_X, which = u / 100, uu = u % 100;
    W = which == 0 ? wq : which == 1 ? wo : which == 2 ? w1 : w2;
    P = which == 0 ? wqp : which == 1 ? wop : which == 2 ? w1p : w2p;
    N = D; KS = 20; ks = uu / 5; nb0 = (uu % 5) * 8;
  } else if (wv_ < PW_B) {
    const int u = wv_ - PW_A; W = w3; P = w3p; N = F; KS = 20; ks = u / 14; nb0 = (u % 14) * 8; if (nb0 == 104) nblk = 4;
  } else if (wv_ < PW_C) {
    const int u = wv_ - PW_B; W = w4; P = w4p; N = D; KS = 54; ks = u / 5; nb0 = (u % 5) * 8;
  } else {
    W = wk; P = wqp; N = DH; KS = 20; ks = wv_ - PW_C; nb0 = 40; nblk = 3; kvt = true;
  }
  u32x4 r[8];
  if (!kvt) {
    const int bb = blk < nblk ? blk : 0;
    const u16* src = W + (size_t)(ks * 32 + kg * 8) * N + (nb0 + bb) * 16 + no2 * 8;
#pragma unroll
    for (int i = 0; i < 8; ++i) r[i] = *(const u32x4*)(src + (size_t)i * N);
  } else {
    const u16* sp[2]; unsigned msk[2];
#pragma unroll
    for (int h2 = 0; h2 < 2; ++h2) {
      const int c4 = blk * 4 + no2 * 2 + h2;
      sp[h2] = (c4 < 5 ? wk + 4 * c4 : wv + 4 * (c4 < 10 ? c4 - 5 : 0)) + (size_t)(ks * 32 + kg * 8) * DH;
      msk[h2] = c4 < 10 ? 0xffffffffu : 0u;
    }
#pragma unroll
    for (int i = 0; i < 8; ++i) {
#pragma unroll
      for (int h2 = 0; h2 < 2; ++h2) {
        const u32x2 e = *(const u32x2*)(sp[h2] + i * DH);
        r[i][2 * h2] = e[0] & msk[h2]; r[i][2 * h2 + 1] = e[1] & msk[h2];
      }
    }
  }
  char* wl = lds + (threadIdx.x >> 6) * (8 * 1152);
  {
    char* d = wl + blk * 1152 + (no2 + 2 * kg) * 144;
#pragma unroll
    for (int i = 0; i < 8; ++i) {
      u32x4 o;
#pragma unroll
      for (int p = 0; p < 4; ++p)
        o[p] = __builtin_amdgcn_perm(r[2 * p + 1][i >> 1], r[2 * p][i >> 1], (i & 1) ? 0x07060302u : 0x05040100u);
      *(u32x4*)(d + i * 16) = o;
    }
  }
  asm volatile("s_waitcnt lgkmcnt(0)" ::: "memory");
#pragma unroll
  for (int bq = 0; bq < 8; ++bq) {
    if (bq < nblk) {
      const u32x4 o = *(const u32x4*)(wl + bq * 1152 + (lane >> 3) * 144 + (lane & 7) * 16);
      *(u32x4*)(P + ((size_t)((nb0 + bq) * KS + ks) * 64 + lane) * 8) = o;
    }
  }
}

constexpr int ACH = 4 * KV_TILE;
constexpr float ATHR = 16.f;
#define SB() __builtin_amdgcn_sched_barrier(0)
#define EXP2(x) __builtin_amdgcn_exp2f(x)

template <bool ONLINE>
DI void attn_pass(const char* __restrict__ KV, char* lds, int lane, int wi, int half, const u32x4 (&q0u)[2], u32x4 (&q1u)[2],
                  f32x16 (&O)[2], float (&mcur)[2]) {
  const int hi = lane >> 5, l32 = lane & 31;
  const unsigned lds0 = lds_addr(lds);
  auto stage = [&](int c, int buf) {
    const char* src = KV + (size_t)(half * 32 + c * 4) * KV_TILE + lane * 16;
    const unsigned dst = __builtin_amdgcn_readfirstlane(lds0 + (buf * 2 + half) * ACH + wi * 1024);
    glds16(src + wi * 1024, dst);
    glds16(src + (wi + 4) * 1024, dst + 4096);
    if (wi < 3 || lane < 16) glds16(src + (wi + 8) * 1024, dst + 8192);
  };
  stage(0, 0); stage(1, 1);
  f32x16 Z;
#pragma unroll
  for (int i = 0; i < 16; ++i) { O[0][i] = 0.f; O[1][i] = 0.f; Z[i] = 0.f; }
  const int koff = lane * 16, k1off = KV_K1 + l32 * 16;
  const int voff = KV_V + (hi * 21 + (l32 < 20 ? l32 : 20)) * 16;
  asm volatile("s_waitcnt vmcnt(0)" ::: "memory");
  __syncthreads();
  const char* hb = lds + half * ACH;
  f32x16 Sx[2];
  bf16x8 k0n, k1n, v0, v1;
  {
    const bf16x8 k0 = *(const bf16x8*)(hb + koff), k1 = *(const bf16x8*)(hb + k1off);
#pragma unroll
    for (int u = 0; u < 2; ++u) {
      Sx[u] = MFMA32(k0, __builtin_bit_cast(bf16x8, q0u[u]), Z);
      Sx[u] = MFMA32(k1, __builtin_bit_cast(bf16x8, q1u[u]), Sx[u]);
    }
    k0n = *(const bf16x8*)(hb + KV_TILE + koff); k1n = *(const bf16x8*)(hb + KV_TILE + k1off);
    v0 = *(const bf16x8*)(hb + voff); v1 = *(const bf16x8*)(hb + voff + 672);
    if (ONLINE) {
#pragma unroll
      for (int u = 0; u < 2; ++u) {
        float mx = fmaxf(fmaxf(Sx[u][0], Sx[u][1]), Sx[u][2]);
#pragma unroll
        for (int i = 3; i < 15; i += 2) mx = fmaxf(fmaxf(mx, Sx[u][i]), Sx[u][i + 1]);
        mx = fmaxf(mx, Sx[u][15]);
        const float mo = fmaxf(mx, __shfl_xor(mx, 32));
        mcur[u] = rbf(mo);
#pragma unroll
        for (int i = 0; i < 16; ++i) Sx[u][i] -= mcur[u];
        q1u[u][2] = hi ? 0u : (pack2(-mcur[u], 0.f) & 0xffffu);
      }
    }
  }
  int bcur = 0;
  for (int c = 0; c < 8; ++c) {
    const int bn1 = (bcur == 2) ? 0 : bcur + 1, bn2 = (bn1 == 2) ? 0 : bn1 + 1;
    if (c + 2 < 8) stage(c + 2, bn2);
    const char* cb = hb + bcur * (2 * ACH);
    const char* nb = hb + bn1 * (2 * ACH);
#pragma unroll
    for (int t = 0; t < 4; ++t) {
      const char* tv = (t < 3) ? cb + (t + 1) * KV_TILE : nb;
      const char* tk = (t < 2) ? cb + (t + 2) * KV_TILE : nb + (t - 2) * KV_TILE;
      f32x16 Sn[2];
#pragma unroll
      for (int u = 0; u < 2; ++u) {
        u32x4 p0, p1;
        SB();
        Sn[u] = MFMA32(k0n, __builtin_bit_cast(bf16x8, q0u[u]), Z);
        SB();
        p0[0] = pack2(EXP2(Sx[u][0]), EXP2(Sx[u][1])); p0[1] = pack2(EXP2(Sx[u][2]), EXP2(Sx[u][3]));
        SB();
        Sn[u] = MFMA32(k1n, __builtin_bit_cast(bf16x8, q1u[u]), Sn[u]);
        if (u == 1) { k0n = *(const bf16x8*)(tk + koff); k1n = *(const bf16x8*)(tk + k1off); }
        SB();
        p0[2] = pack2(EXP2(Sx[u][4]), EXP2(Sx[u][5])); p0[3] = pack2(EXP2(Sx[u][6]), EXP2(Sx[u][7]));
        SB();
        O[u] = MFMA32(v0, __builtin_bit_cast(bf16x8, p0), O[u]);
        if (u == 1) v0 = *(const bf16x8*)(tv + voff);
        SB();
        p1[0] = pack2(EXP2(Sx[u][8]), EXP2(Sx[u][9])); p1[1] = pack2(EXP2(Sx[u][10]), EXP2(Sx[u][11]));
        p1[2] = pack2(EXP2(Sx[u][12]), EXP2(Sx[u][13])); p1[3] = pack2(EXP2(Sx[u][14]), EXP2(Sx[u][15]));
        SB();
        O[u] = MFMA32(v1, __builtin_bit_cast(bf16x8, p1), O[u]);
        if (u == 1) v1 = *(const bf16x8*)(tv + voff + 672);
        SB();
      }
#pragma unroll
      for (int u = 0; u < 2; ++u) {
        if (ONLINE) {
          float mx = fmaxf(fmaxf(Sn[u][0], Sn[u][1]), Sn[u][2]);
#pragma unroll
          for (int i = 3; i < 15; i += 2) mx = fmaxf(fmaxf(mx, Sn[u][i]), Sn[u][i + 1]);
          mx = fmaxf(mx, Sn[u][15]);
          if (!(c == 7 && t == 3) && __any(mx > ATHR)) {
            const float mo = fmaxf(mx, __shfl_xor(mx, 32));
            const float mnew = rbf(mcur[u] + fmaxf(mo, 0.f));
            const float delta = mnew - mcur[u];
            const float sc = EXP2(-delta);
#pragma unroll
            for (int i = 0; i < 16; ++i) { Sn[u][i] -= delta; O[u][i] *= sc; }
            mcur[u] = mnew;
            q1u[u][2] = hi ? 0u : (pack2(-mnew, 0.f) & 0xffffu);
          }
        }
        Sx[u] = Sn[u];
      }
    }
    asm volatile("s_waitcnt vmcnt(0)" ::: "memory");
    __syncthreads();
    bcur = bn1;
  }
}

__global__ __launch_bounds__(512, 2) void attn_kernel(const u16* __restrict__ Q, const char* __restrict__ KV,
                                                      const float* __restrict__ kmax, u16* __restrict__ Op) {
  __shared__ __attribute__((aligned(16))) char lds[6 * ACH];
  const int tid = threadIdx.x, lane = tid & 63;
  const int w = __builtin_amdgcn_readfirstlane(tid >> 6);
  const int hi = lane >> 5, l32 = lane & 31;
  const int b = blockIdx.x, xc = b & 7, bi = b >> 3;
  const int tb = xc * 8 + (bi >> 2), hg = bi & 3;
  const int head0 = hg * 8 + (w & 3) * 2, half = w >> 2, wi = w & 3;
  const int tok = tb * 32 + l32;
  float kk;
  {
    const f32x2 ka = *(const f32x2*)(kmax + lane * 2), kb = *(const f32x2*)(kmax + 128 + lane * 2);
    float ma = fmaxf(ka[0], ka[1]), mb = fmaxf(kb[0], kb[1]);
#pragma unroll
    for (int o = 1; o < 64; o <<= 1) { ma = fmaxf(ma, __shfl_xor(ma, o)); mb = fmaxf(mb, __shfl_xor(mb, o)); }
    kk = ma + mb;
  }
  u32x4 q0u[2], q1u[2];
  float mcur[2];
#pragma unroll
  for (int u = 0; u < 2; ++u) {
    const u16* qp = Q + (size_t)tok * D + (head0 + u) * DH;
    const u32x2 a = *(const u32x2*)(qp + 8 * hi), c = *(const u32x2*)(qp + 8 * hi + 4);
    q0u[u] = (u32x4){a[0], a[1], c[0], c[1]};
    u32x2 e = *(const u32x2*)(qp + 16);
    q1u[u] = (u32x4){hi ? 0u : e[0], hi ? 0u : e[1], 0u, 0u};
    float qq = 0.f;
#pragma unroll
    for (int i = 0; i < 4; ++i) { qq += bflo(q0u[u][i]) * bflo(q0u[u][i]) + bfhi(q0u[u][i]) * bfhi(q0u[u][i]); }
#pragma unroll
    for (int i = 0; i < 2; ++i) { qq += bflo(q1u[u][i]) * bflo(q1u[u][i]) + bfhi(q1u[u][i]) * bfhi(q1u[u][i]); }
    qq += __shfl_xor(qq, 32);
    mcur[u] = rbf(__builtin_sqrtf(qq * kk) * 1.02f + 1e-30f);
    q1u[u][2] = hi ? 0u : (pack2(-mcur[u], 0.f) & 0xffffu);
  }
  f32x16 O[2];
  attn_pass<false>(KV, lds, lane, wi, half, q0u, q1u, O, mcur);
  {
    const float l0 = hi ? O[0][8] : O[0][12], l1 = hi ? O[1][8] : O[1][12];
    const int bad = !(l0 >= 8.6736174e-19f && l0 < 1e30f && l1 >= 8.6736174e-19f && l1 < 1e30f);
    if (__syncthreads_or(bad)) {
      q1u[0][2] = 0u; q1u[1][2] = 0u;
      attn_pass<true>(KV, lds, lane, wi, half, q0u, q1u, O, mcur);
    }
  }
  float* cx = (float*)lds;
  if (half == 1) {
#pragma unroll
    for (int u = 0; u < 2; ++u) {
#pragma unroll
      for (int i = 0; i < 16; ++i) cx[((wi * 2 + u) * 17 + i) * 64 + lane] = O[u][i];
      cx[((wi * 2 + u) * 17 + 16) * 64 + lane] = mcur[u];
    }
  }
  __syncthreads();
  if (half == 0) {
#pragma unroll
    for (int u = 0; u < 2; ++u) {
      const float m2 = cx[((wi * 2 + u) * 17 + 16) * 64 + lane];
      const float mm = fmaxf(mcur[u], m2);
      const float f1 = EXP2(mcur[u] - mm), f2 = EXP2(m2 - mm);
#pragma unroll
      for (int i = 0; i < 16; ++i) O[u][i] = O[u][i] * f1 + cx[((wi * 2 + u) * 17 + i) * 64 + lane] * f2;
      const float l = hi ? O[u][8] : O[u][12];
      const float rl = 1.f / l;
      const int col0 = (head0 + u) * DH + 4 * hi;
#pragma unroll
      for (int q = 0; q < 3; ++q) {
        if (q < 2 || hi == 0) {
          u32x2 o = {pack2(O[u][4 * q] * rl, O[u][4 * q + 1] * rl), pack2(O[u][4 * q + 2] * rl, O[u][4 * q + 3] * rl)};
          *(u32x2*)(Op + apack_off(tok, col0 + 8 * q, 20)) = o;
        }
      }
    }
  }
}

enum { EPI_WO = 0, EPI_W1 = 1, EPI_W2 = 2, EPI_W3 = 3, EPI_W4 = 4, EPI_QKV = 5 };

template <int N> DI void wait_vm_barrier() {
  asm volatile("s_waitcnt vmcnt(%0)\n\ts_barrier" :: "i"(N) : "memory");
}

template <int I, int CNT> DI void drain_tail() {
  wait_vm_barrier<I * CNT>();
  if constexpr (I > 0) drain_tail<I - 1, CNT>();
}

template <int EPI> struct GCfg {
  static constexpr int MPW = (EPI == EPI_W3) ? 2 : 1;
  static constexpr int NBMAX = (EPI == EPI_W3) ? 7 : (EPI == EPI_QKV) ? 6 : 5;
  static constexpr int PRE = (EPI == EPI_W3) ? 2 : 3;
  static constexpr int LDSB = (PRE + 1) * 2 * (4 * MPW + NBMAX) * 1024 + 1024;
};

template <int EPI, int NB, int NS, int NC>
DI void gemm_compute(char* lds, const u16* __restrict__ r1, const u16* __restrict__ r2, const float* __restrict__ stin,
                     float* __restrict__ stout, u16* __restrict__ out, char* __restrict__ KVo, int w, int mblk0, int nblk0,
                     int nbcnt, int nt) {
  constexpr bool LN = (EPI == EPI_W1 || EPI == EPI_W3 || EPI == EPI_QKV);
  constexpr bool NEEDST = (EPI == EPI_W1 || EPI == EPI_W2 || EPI == EPI_W3 || EPI == EPI_QKV);
  constexpr int MPW = GCfg<EPI>::MPW, MBT = 4 * MPW;
  constexpr int KST = (EPI == EPI_W4) ? 54 : 20, NST = KST / 2;
  constexpr int PPK = MBT + NB, RING = GCfg<EPI>::PRE + 1, SLOT = 2 * PPK * 1024;
  const int lane = threadIdx.x & 63, g = lane >> 4, r16 = lane & 15;
  const int wm = w & 3;

  f32x4 stv[MPW][2];
  u32x2 ra[MPW][NC], rb[MPW][NC];
#pragma unroll
  for (int o = 0; o < MPW; ++o) {
    const int tok = (mblk0 + wm * MPW + o) * 16 + r16;
    if (NEEDST) {
#pragma unroll
      for (int q = 0; q < 2; ++q) stv[o][q] = *(const f32x4*)(stin + (size_t)(tok - r16) * 32 + q * 256 + lane * 4);
    }
#pragma unroll
    for (int nb = 0; nb < NC; ++nb) {
      const int col = (nblk0 + NS + nb) * 16 + 4 * g;
      if (EPI == EPI_WO) ra[o][nb] = *(const u32x2*)(r1 + (size_t)tok * D + col);
      if (EPI == EPI_W2 || EPI == EPI_W4) ra[o][nb] = *(const u32x2*)(r1 + apack_off(tok, col, 20));
      if (EPI == EPI_W4) rb[o][nb] = *(const u32x2*)(r2 + apack_off(tok, col, 20));
    }
  }
  __builtin_amdgcn_sched_barrier(0);

  f32x4 acc[MPW][NC], cs = {0.f, 0.f, 0.f, 0.f};
#pragma unroll
  for (int nb = 0; nb < NC; ++nb)
#pragma unroll
    for (int o = 0; o < MPW; ++o) acc[o][nb] = (f32x4){0.f, 0.f, 0.f, 0.f};
  const u32x4 ones4 = {0x3F803F80u, 0x3F803F80u, 0x3F803F80u, 0x3F803F80u};
  const bf16x8 ones = __builtin_bit_cast(bf16x8, ones4);
  const int csj = MBT + (w < NB ? w : 0);

  int slot = 0;
  for (int s = 0; s < NST; ++s) {
    asm volatile("s_waitcnt lgkmcnt(0)\n\ts_barrier" ::: "memory");
    const char* sb = lds + slot * SLOT + lane * 16;
    slot = (slot == RING - 1) ? 0 : slot + 1;
    bf16x8 af[2][MPW], bfr[2][NC], cf[2];
#pragma unroll
    for (int kl = 0; kl < 2; ++kl) {
#pragma unroll
      for (int o = 0; o < MPW; ++o) af[kl][o] = *(const bf16x8*)(sb + (kl * PPK + wm * MPW + o) * 1024);
#pragma unroll
      for (int nb = 0; nb < NC; ++nb) bfr[kl][nb] = *(const bf16x8*)(sb + (kl * PPK + MBT + NS + nb) * 1024);
      if (LN) cf[kl] = *(const bf16x8*)(sb + (kl * PPK + csj) * 1024);
    }
    __builtin_amdgcn_sched_barrier(0);
#pragma unroll
    for (int kl = 0; kl < 2; ++kl) {
#pragma unroll
      for (int nb = 0; nb < NC; ++nb)
#pragma unroll
        for (int o = 0; o < MPW; ++o) acc[o][nb] = MFMA16(bfr[kl][nb], af[kl][o], acc[o][nb]);
      if (LN) cs = MFMA16(cf[kl], ones, cs);
    }
  }
  f32x4 c[NC];
  if (LN) {
    float* csl = (float*)(lds + GCfg<EPI>::LDSB - 1024);
    if (r16 == 0 && w < NB) *(f32x4*)(csl + w * 16 + 4 * g) = cs;
    __syncthreads();
#pragma unroll
    for (int nb = 0; nb < NC; ++nb) c[nb] = *(const f32x4*)(csl + (NS + nb) * 16 + 4 * g);
  }

#pragma unroll
  for (int o = 0; o < MPW; ++o) {
    const int tok = (mblk0 + wm * MPW + o) * 16 + r16;
    float mean = 0.f, rs = 0.f;
    if (NEEDST) {
      float t1 = 0.f, t2 = 0.f;
#pragma unroll
      for (int q = 0; q < 2; ++q) {
        float u1 = stv[o][q][0] + stv[o][q][2], u2 = stv[o][q][1] + stv[o][q][3];
#pragma unroll
        for (int of = 1; of < 8; of <<= 1) { u1 += __shfl_xor(u1, of); u2 += __shfl_xor(u2, of); }
        const float v1 = __shfl(u1, (r16 & 7) * 8), v2 = __shfl(u2, (r16 & 7) * 8);
        if ((r16 >> 3) == q) { t1 = v1; t2 = v2; }
      }
      mean = t1 * (1.f / D);
      const float var = fmaxf(t2 * (1.f / D) - mean * mean, 0.f);
      rs = __builtin_amdgcn_rsqf(var + LN_EPS);
      if (EPI == EPI_W1) rs = rs * __builtin_amdgcn_rsqf(var * rs * rs + LN_EPS);
    }
    float q1 = 0.f, q2 = 0.f;
#pragma unroll
    for (int nb = 0; nb < NC; ++nb) {
      const int col = (nblk0 + NS + nb) * 16 + 4 * g;
      float z[4];
      if (EPI == EPI_WO) {
        const float xr[4] = {bflo(ra[o][nb][0]), bfhi(ra[o][nb][0]), bflo(ra[o][nb][1]), bfhi(ra[o][nb][1])};
#pragma unroll
        for (int r = 0; r < 4; ++r) { z[r] = rbf(xr[r] + rbf(acc[o][nb][r])); q1 += z[r]; q2 += z[r] * z[r]; }
        u32x2 ov = {pack2(z[0], z[1]), pack2(z[2], z[3])};
        *(u32x2*)(out + apack_off(tok, col, 20)) = ov;
      } else if (EPI == EPI_W1) {
#pragma unroll
        for (int r = 0; r < 4; ++r) z[r] = gelu_t(rbf((acc[o][nb][r] - mean * c[nb][r]) * rs));
        u32x2 ov = {pack2(z[0], z[1]), pack2(z[2], z[3])};
        const int grp = nt >> 1;
        const int sh = (grp == 0) ? 1 : (grp == 1) ? -1 : (grp == 2) ? 2 : -2;
        const int t2 = tok + sh;
        if (t2 >= 0 && t2 < S) *(u32x2*)(out + apack_off(t2, col, 20)) = ov;
        if (tok - sh < 0 || tok - sh >= S) *(u32x2*)(out + apack_off(tok, col, 20)) = (u32x2){0u, 0u};
      } else if (EPI == EPI_W2) {
        const float hr[4] = {bflo(ra[o][nb][0]), bfhi(ra[o][nb][0]), bflo(ra[o][nb][1]), bfhi(ra[o][nb][1])};
#pragma unroll
        for (int r = 0; r < 4; ++r) { z[r] = rbf(rbf((hr[r] - mean) * rs) + rbf(acc[o][nb][r])); q1 += z[r]; q2 += z[r] * z[r]; }
        u32x2 ov = {pack2(z[0], z[1]), pack2(z[2], z[3])};
        *(u32x2*)(out + apack_off(tok, col, 20)) = ov;
      } else if (EPI == EPI_W3) {
#pragma unroll
        for (int r = 0; r < 4; ++r) z[r] = gelu_t(rbf((acc[o][nb][r] - mean * c[nb][r]) * rs));
        u32x2 ov = {pack2(z[0], z[1]), pack2(z[2], z[3])};
        if (NS + nb < nbcnt) *(u32x2*)(out + apack_off(tok, col, 54)) = ov;
      } else if (EPI == EPI_QKV) {
        if (NS + nb < 5) {
#pragma unroll
          for (int r = 0; r < 4; ++r) z[r] = (acc[o][nb][r] - mean * c[nb][r]) * (rs * QSCALE);
          u32x2 ov = {pack2(z[0], z[1]), pack2(z[2], z[3])};
          *(u32x2*)(out + (size_t)tok * D + col) = ov;
        } else {
#pragma unroll
          for (int r = 0; r < 4; ++r) z[r] = (acc[o][nb][r] - mean * c[nb][r]) * rs;
          const int c4 = nt * 4 + g;
          if (nt < 2) {
            float nn = 0.f;
            if (c4 < 5) {
#pragma unroll
              for (int r = 0; r < 4; ++r) { const float t = rbf(z[r]); nn += t * t; }
            }
            nn += __shfl_xor(nn, 16); nn += __shfl_xor(nn, 32);
#pragma unroll
            for (int of = 1; of < 16; of <<= 1) nn = fmaxf(nn, __shfl_xor(nn, of));
            if (lane == 0) stout[nt * 128 + (tok >> 4)] = nn;
          }
          char* tb = KVo + (size_t)(tok >> 5) * KV_TILE;
          const int key = tok & 31;
          if (c4 < 4) {
            u32x2 ov = {pack2(z[0], z[1]), pack2(z[2], z[3])};
            *(u32x2*)(tb + (key + 32 * (c4 >> 1)) * 16 + (c4 & 1) * 8) = ov;
          } else if (c4 == 4) {
            *(u32x4*)(tb + KV_K1 + key * 16) = (u32x4){pack2(z[0], z[1]), pack2(z[2], z[3]), 0x00003F80u, 0u};
          } else if (c4 < 10) {
            const int sk = key >> 4, hh = (key >> 2) & 1, j = ((key >> 3) & 1) * 4 + (key & 3);
            u16* vb = (u16*)(tb + KV_V) + ((sk * 2 + hh) * 21 + 4 * (c4 - 5)) * 8 + j;
#pragma unroll
            for (int r = 0; r < 4; ++r) vb[r * 8] = (u16)(pack2(z[r], 0.f) & 0xffffu);
            if (c4 == 9) vb[4 * 8] = (u16)0x3F80;
          }
        }
      } else {
        const float hr[4] = {bflo(ra[o][nb][0]), bfhi(ra[o][nb][0]), bflo(ra[o][nb][1]), bfhi(ra[o][nb][1])};
        const float xr[4] = {bflo(rb[o][nb][0]), bfhi(rb[o][nb][0]), bflo(rb[o][nb][1]), bfhi(rb[o][nb][1])};
#pragma unroll
        for (int r = 0; r < 4; ++r) z[r] = hr[r] + rbf(xr[r] + rbf(acc[o][nb][r]));
        u32x2 ov = {pack2(z[0], z[1]), pack2(z[2], z[3])};
        *(u32x2*)(out + (size_t)tok * D + col) = ov;
      }
    }
    if (EPI == EPI_WO || EPI == EPI_W2) {
      q1 += __shfl_xor(q1, 16); q2 += __shfl_xor(q2, 16);
      q1 += __shfl_xor(q1, 32); q2 += __shfl_xor(q2, 32);
      if (lane < 16) *(f32x2*)(stout + (size_t)tok * 32 + (nt * 2 + (NS > 0 ? 1 : 0)) * 2) = (f32x2){q1, q2};
    }
  }
}

template <int EPI, int NB>
DI void gemm_body(char* lds, const u16* __restrict__ A, const u16* __restrict__ Bp, const u16* __restrict__ r1,
                  const u16* __restrict__ r2, const float* __restrict__ stin, float* __restrict__ stout, u16* __restrict__ out,
                  char* __restrict__ KVo) {
  constexpr int MPW = GCfg<EPI>::MPW, MBT = 4 * MPW;
  constexpr int KST = (EPI == EPI_W4) ? 54 : 20, NST = KST / 2;
  constexpr int PPK = MBT + NB;
  constexpr int PRE = GCfg<EPI>::PRE, RING = PRE + 1;
  constexpr int SLOT = 2 * PPK * 1024;
  const int lane = threadIdx.x & 63;
  const int w = __builtin_amdgcn_readfirstlane(threadIdx.x >> 6);
  const int b = blockIdx.x, xc = b & 7, bi = b >> 3;
  int mblk0, nblk0, nbcnt, nt;
  if (EPI == EPI_W3) {
    const int mt = xc * 2 + (bi >> 4); nt = bi & 15;
    mblk0 = mt * 8;
    nblk0 = nt < 12 ? 7 * nt : 84 + 6 * (nt - 12);
    nbcnt = nt < 12 ? 7 : 6;
  } else {
    const int mt = xc * 4 + (bi >> 3); nt = bi & 7;
    mblk0 = mt * 4; nblk0 = nt * 5; nbcnt = 5;
  }
  if (w >= 8) {
    const int kl = w & 1;
    const unsigned lds0 = lds_addr(lds) + kl * PPK * 1024;
    const u16* Ab = A + ((size_t)mblk0 * KST + kl) * 512 + lane * 8;
    const u16* Bb = Bp + ((size_t)nblk0 * KST + kl) * 512 + lane * 8;
    auto run = [&](auto J0c, auto CNTc) {
      constexpr int J0 = decltype(J0c)::value, CNT = decltype(CNTc)::value;
      auto issue = [&](int s, int slot) {
#pragma unroll
        for (int j = J0; j < J0 + CNT; ++j) {
          const unsigned dst = lds0 + slot * SLOT + j * 1024;
          if (j < MBT) glds16(Ab + ((size_t)j * KST + 2 * s) * 512, dst);
          else {
            const int nb = j - MBT;
            const int nn = (EPI == EPI_W3) ? (nb < nbcnt ? nb : nbcnt - 1) : (EPI == EPI_QKV && nb == 5) ? 40 + nt - nblk0 : nb;
            glds16(Bb + ((size_t)nn * KST + 2 * s) * 512, dst);
          }
        }
      };
#pragma unroll
      for (int s = 0; s < PRE; ++s) issue(s, s);
      int slot = PRE;
      for (int s = 0; s < NST - PRE; ++s) {
        wait_vm_barrier<(PRE - 1) * CNT>();
        issue(s + PRE, slot);
        slot = (slot == RING - 1) ? 0 : slot + 1;
      }
      drain_tail<PRE - 1, CNT>();
    };
    constexpr int H0 = (PPK + 1) / 2;
    if (w < 10) run(std::integral_constant<int, 0>{}, std::integral_constant<int, H0>{});
    else run(std::integral_constant<int, H0>{}, std::integral_constant<int, PPK - H0>{});
    return;
  }
  constexpr int N0 = (NB + 1) / 2;
  if (w < 4) gemm_compute<EPI, NB, 0, N0>(lds, r1, r2, stin, stout, out, KVo, w, mblk0, nblk0, nbcnt, nt);
  else gemm_compute<EPI, NB, N0, NB - N0>(lds, r1, r2, stin, stout, out, KVo, w, mblk0, nblk0, nbcnt, nt);
}

template <int EPI>
__global__ __launch_bounds__(768, 1) void gemm_kernel(const u16* __restrict__ A, const u16* __restrict__ Bp, const u16* __restrict__ r1,
                                                      const u16* __restrict__ r2, const float* __restrict__ stin,
                                                      float* __restrict__ stout, u16* __restrict__ out, char* __restrict__ KVo) {
  __shared__ __attribute__((aligned(16))) char lds[GCfg<EPI>::LDSB];
  if (EPI == EPI_QKV) {
    if (((blockIdx.x >> 3) & 7) < 3) gemm_body<EPI, 6>(lds, A, Bp, r1, r2, stin, stout, out, KVo);
    else gemm_body<EPI, 5>(lds, A, Bp, r1, r2, stin, stout, out, KVo);
  } else {
    gemm_body<EPI, GCfg<EPI>::NBMAX>(lds, A, Bp, r1, r2, stin, stout, out, KVo);
  }
}

extern "C" void kernel_launch(void* const* d_in, const int* in_sizes, int n_in, void* d_out, int out_size, void* d_ws,
                              size_t ws_size, hipStream_t stream) {
  const u16* x = (const u16*)d_in[0];
  const u16* wq = (const u16*)d_in[1];
  const u16* wk = (const u16*)d_in[2];
  const u16* wv = (const u16*)d_in[3];
  const u16* wo = (const u16*)d_in[4];
  const u16* w1 = (const u16*)d_in[6];
  const u16* w2 = (const u16*)d_in[7];
  const u16* w3 = (const u16*)d_in[8];
  const u16* w4 = (const u16*)d_in[9];
  u16* out = (u16*)d_out;
  char* ws = (char*)d_ws;
  size_t off = 0;
  auto take = [&](size_t bytes) { char* p = ws + off; off += (bytes + 4095) & ~(size_t)4095; return p; };
  u16* Qb = (u16*)take((size_t)S * D * 2);
  char* KVb = take((size_t)64 * KV_TILE + 1024);
  u16* Ob = (u16*)take((size_t)S * D * 2);
  u16* hb = (u16*)take((size_t)S * D * 2);
  u16* hhb = (u16*)take((size_t)S * D * 2);
  u16* x1b = (u16*)take((size_t)S * D * 2);
  u16* gb = (u16*)take((size_t)S * F * 2);
  u16* wop = (u16*)take((size_t)D * D * 2);
  u16* w1p = (u16*)take((size_t)D * D * 2);
  u16* w2p = (u16*)take((size_t)D * D * 2);
  u16* w3p = (u16*)take((size_t)D * F * 2);
  u16* w4p = (u16*)take((size_t)D * F * 2);
  float* sth = (float*)take((size_t)S * 32 * 4);
  float* stx = (float*)take((size_t)S * 32 * 4);
  float* kmx = (float*)take(1024);
  u16* xp = (u16*)take((size_t)S * D * 2);
  u16* wqp = (u16*)take((size_t)D * 688 * 2);
  float* st0 = (float*)take((size_t)S * 32 * 4);

  pack_kernel<<<(PW_D + 3) / 4, 256, 0, stream>>>(x, wq, wk, wv, wo, w1, w2, w3, w4, xp, st0, wqp, wop, w1p, w2p, w3p, w4p);
  gemm_kernel<EPI_QKV><<<256, 768, 0, stream>>>(xp, wqp, nullptr, nullptr, st0, kmx, Qb, KVb);
  attn_kernel<<<256, 512, 0, stream>>>(Qb, KVb, kmx, Ob);
  gemm_kernel<EPI_WO><<<256, 768, 0, stream>>>(Ob, wop, x, nullptr, nullptr, sth, hb, nullptr);
  gemm_kernel<EPI_W1><<<256, 768, 0, stream>>>(hb, w1p, nullptr, nullptr, sth, nullptr, hhb, nullptr);
  gemm_kernel<EPI_W2><<<256, 768, 0, stream>>>(hhb, w2p, hb, nullptr, sth, stx, x1b, nullptr);
  gemm_kernel<EPI_W3><<<256, 768, 0, stream>>>(x1b, w3p, nullptr, nullptr, stx, nullptr, gb, nullptr);
  gemm_kernel<EPI_W4><<<256, 768, 0, stream>>>(gb, w4p, hb, x1b, nullptr, nullptr, out, nullptr);
}
```
